```python
import torch

def _softmax(x, axis=-1):
    return torch.softmax(x, dim=axis)
def _gelu(x):
    return torch.nn.functional.gelu(x, approximate="tanh")
def _silu(x):
    return torch.nn.functional.silu(x)
def _sigmoid(x):
    return torch.sigmoid(x)
def _layernorm(x, axis=-1, eps=1e-5):
    m = x.mean(dim=axis, keepdim=True)
    v = x.var(dim=axis, keepdim=True, unbiased=False)
    return (x - m) / torch.sqrt(v + eps)
def _scatter_add(tgt, idx, src, axis=0):
    return tgt.clone().index_add_(axis, idx, src)
def _segment_sum(src, seg, n):
    out = torch.zeros((n,) + src.shape[1:], dtype=src.dtype)
    return out.index_add_(0, seg, src)
def _pad(x, pw):
    flat = tuple(v for pair in reversed(pw) for v in pair)
    return torch.nn.functional.pad(x, flat)

kernel_name = 'concat_heads__ssm_conv_s5__mha_block__prompt'

def setup_inputs():

    _w = (lambda a: (lambda t: t.to(torch.bfloat16) if t.is_floating_point() else t)(torch.as_tensor(a)))
    def _A_setup():
        B, S, D = 1, 2048, 448
        P = D
        Lambda_re = -0.5 * (lambda _x: torch.exp(torch.as_tensor(_x)))((lambda *s: torch.randn(*s, dtype=torch.bfloat16) * 0.4)(P))
        Lambda_im = (lambda *s: torch.randn(*s, dtype=torch.bfloat16) * 0.8)(P) * 1.0
        B_re = (lambda *s: torch.randn(*s, dtype=torch.bfloat16) * 0.4)(P, D) * (D ** -0.5)
        B_im = (lambda *s: torch.randn(*s, dtype=torch.bfloat16) * 0.4)(P, D) * (D ** -0.5)
        C_re = (lambda *s: torch.randn(*s, dtype=torch.bfloat16) * 0.4)(D, P) * (P ** -0.5)
        C_im = (lambda *s: torch.randn(*s, dtype=torch.bfloat16) * 0.4)(D, P) * (P ** -0.5)
        log_dt = (lambda *s: torch.randn(*s, dtype=torch.bfloat16) * 0.4)(P) * 0.3 - 2.0
        Dskip = (lambda *s: torch.randn(*s, dtype=torch.bfloat16) * 0.05)(D)
        return {
            "x": _w((lambda *s: torch.randn(*s, dtype=torch.bfloat16) * 0.05)(B, S, D)),
            "Lambda_re": _w(Lambda_re),
            "Lambda_im": _w(Lambda_im),
            "B_re": _w(B_re),
            "B_im": _w(B_im),
            "C_re": _w(C_re),
            "C_im": _w(C_im),
            "log_dt": _w(log_dt),
            "Dskip": _w(Dskip),
        }
    _A_in = _A_setup()

    def _B_setup():
        B, S, D, H = 1, 2048, 448, 16
        return {
            "x": _w((lambda *s: torch.randn(*s, dtype=torch.bfloat16) * 0.2)(B, S, D)),
            "wqkv": _w((lambda *s: torch.randn(*s, dtype=torch.bfloat16) * 0.2)(D, 3 * D)),
            "wo": _w((lambda *s: torch.randn(*s, dtype=torch.bfloat16) * 0.05)(D, D)),
        }
    _B_in = _B_setup()

    return {
        "A_x": _A_in["x"],
        "A_Lambda_re": _A_in["Lambda_re"],
        "A_Lambda_im": _A_in["Lambda_im"],
        "A_B_re": _A_in["B_re"],
        "A_B_im": _A_in["B_im"],
        "A_C_re": _A_in["C_re"],
        "A_C_im": _A_in["C_im"],
        "A_log_dt": _A_in["log_dt"],
        "A_Dskip": _A_in["Dskip"],
        "B_x": _B_in["x"],
        "B_wqkv": _B_in["wqkv"],
        "B_wo": _B_in["wo"],
    }

def reference(A_x, A_Lambda_re, A_Lambda_im, A_B_re, A_B_im, A_C_re, A_C_im, A_log_dt, A_Dskip, B_x, B_wqkv, B_wo):
    def _A_ref(x, Lambda_re, Lambda_im, B_re, B_im, C_re, C_im, log_dt, Dskip):
        Bsz, L, H = x.shape
        Lam = Lambda_re + 1j * Lambda_im
        Bmat = B_re + 1j * B_im
        Cmat = C_re + 1j * C_im
        dt = torch.exp(log_dt)
        Lam_bar = torch.exp(dt * Lam)
        B_bar = ((Lam_bar - 1.0) / Lam)[:, None] * Bmat
        Bu = torch.einsum("bld,pd->blp", x + 0j, B_bar)
        h = torch.zeros_like(Bu[:, 0, :])
        hs = []
        for t in range(L):
            h = Lam_bar[None, :] * h + Bu[:, t, :]
            hs.append(h)
        hseq = torch.stack(hs, 1)
        y = 2.0 * torch.einsum("blp,dp->bld", hseq, Cmat).real + x * Dskip[None, None, :]
        return y

    def _B_ref(x, wqkv, wo):
        B, S, D = x.shape
        H = 16
        Dh = D // H
        qkv = (lambda x, *d: x.permute(*d))((x @ wqkv).reshape(B, S, 3, H, Dh), 2, 0, 3, 1, 4)
        q, k, v = qkv[0], qkv[1], qkv[2]
        scores = torch.einsum("bhqd,bhkd->bhqk", q, k) * (Dh ** -0.5)
        p = _softmax(scores, axis=-1)
        o = torch.einsum("bhqk,bhkd->bhqd", p, v)
        return (lambda x, *d: x.permute(*d))(o, 0, 2, 1, 3).reshape(B, S, D) @ wo

    a = _A_ref(A_x, A_Lambda_re, A_Lambda_im, A_B_re, A_B_im, A_C_re, A_C_im, A_log_dt, A_Dskip)
    b = _B_ref(B_x, B_wqkv, B_wo)
    return torch.concatenate([a, b], axis=-1)
```

```cpp
#include <hip/hip_runtime.h>
#include <stdint.h>

typedef short bf16x8 __attribute__((ext_vector_type(8)));
typedef short s16x4 __attribute__((ext_vector_type(4)));
typedef float f32x16 __attribute__((ext_vector_type(16)));
typedef float f32x4 __attribute__((ext_vector_type(4)));
typedef float f32x2 __attribute__((ext_vector_type(2)));
typedef unsigned u32x4 __attribute__((ext_vector_type(4)));
typedef unsigned u32x2 __attribute__((ext_vector_type(2)));
typedef int i32x4 __attribute__((ext_vector_type(4)));
typedef unsigned long long u64;
typedef unsigned short u16;
#define GAS __attribute__((address_space(1)))
#define LAS __attribute__((address_space(3)))
typedef LAS char* lptr;

constexpr int SEQ = 2048, DM = 448, NH = 16, DH = 28, OUTW = 896;

constexpr size_t WS_EP = 0;
constexpr size_t WS_TMO = 256;
constexpr size_t WS_FLAG = 4096;
constexpr int F_KV = 0, F_E = 256, F_H = 512, F_O = 768;
constexpr size_t WS_KV = 1u << 20;
constexpr size_t WS_E = 5u << 20;
constexpr size_t WS_HF = 6u << 20;
constexpr size_t WS_OF = 10u << 20;

constexpr int L_SLOT = 28672;
constexpr int L_WO = 65536;
constexpr int L_HT = 126976;
constexpr int L_Q = 143360;
constexpr int L_MISC = 151552;
constexpr int L_SEG = L_MISC, L_M = L_MISC + 2048, L_CNT = L_MISC + 4096;
constexpr int LDS_BYTES = L_MISC + 4096 + 64;

#define MFMA(a, b, c) __builtin_amdgcn_mfma_f32_32x32x16_bf16(a, b, c, 0, 0, 0)
#define RLX_AGENT __ATOMIC_RELAXED, __HIP_MEMORY_SCOPE_AGENT

typedef __bf16 bf16x2_t __attribute__((ext_vector_type(2)));
__device__ __forceinline__ unsigned cvtpk(float lo, float hi) { const f32x2 v = {lo, hi}; return __builtin_bit_cast(unsigned, __builtin_convertvector(v, bf16x2_t)); }
__device__ __forceinline__ float bf2f(u16 b) { return __uint_as_float((unsigned)b << 16); }
__device__ __forceinline__ i32x4 make_srd(const void* p, unsigned bytes) {
    const u64 a = (u64)p; i32x4 r; r[0] = (int)(unsigned)a; r[1] = (int)((unsigned)(a >> 32) & 0xffffu); r[2] = (int)bytes; r[3] = 0x00020000; return r;
}
__device__ __forceinline__ void dma16(i32x4 srd, unsigned voff, unsigned soff, unsigned ldsaddr) {
    unsigned keep;
    asm volatile("s_mov_b32 %0, m0\n\ts_mov_b32 m0, %4\n\ts_nop 0\n\tbuffer_load_dwordx4 %1, %2, %3 offen lds\n\ts_mov_b32 m0, %0"
                 : "=&s"(keep) : "v"(voff), "s"(srd), "s"(soff), "s"(ldsaddr) : "memory");
}
__device__ __forceinline__ bf16x8 lds_b128(lptr p) { return *(const LAS bf16x8*)p; }
typedef short v4i16_t __attribute__((ext_vector_type(4)));
__device__ __forceinline__ s16x4 lds_tr(lptr p) { return __builtin_bit_cast(s16x4, __builtin_amdgcn_ds_read_tr16_b64_v4i16((LAS v4i16_t*)p)); }
__device__ __forceinline__ bf16x8 lds_tr2(lptr p0, lptr p1) { const s16x4 a = lds_tr(p0), b = lds_tr(p1); return (bf16x8){a[0], a[1], a[2], a[3], b[0], b[1], b[2], b[3]}; }
__device__ __forceinline__ bf16x8 pack8(const f32x16& a, int s) {
    u32x4 r; r[0] = cvtpk(a[8 * s + 0], a[8 * s + 1]); r[1] = cvtpk(a[8 * s + 2], a[8 * s + 3]); r[2] = cvtpk(a[8 * s + 4], a[8 * s + 5]); r[3] = cvtpk(a[8 * s + 6], a[8 * s + 7]);
    return __builtin_bit_cast(bf16x8, r);
}
__device__ __forceinline__ void st16_sc1(__amdgpu_buffer_rsrc_t rs, unsigned off, u32x4 v) { __builtin_amdgcn_raw_buffer_store_b128(v, rs, off, 0, 16); }
__device__ __forceinline__ u32x4 ld16_sc1(__amdgpu_buffer_rsrc_t rs, unsigned off) { return __builtin_amdgcn_raw_buffer_load_b128(rs, off, 0, 16); }

#define WAIT_BAR(N) asm volatile("s_waitcnt vmcnt(" #N ") lgkmcnt(0)\n\ts_barrier" ::: "memory")
#define WAIT_VM0() asm volatile("s_waitcnt vmcnt(0)" ::: "memory")

constexpr unsigned SPIN_CAP = 1u << 18;
__device__ __forceinline__ void wait_flags(GAS u64* flag, bool active, u64 tag, GAS unsigned* tmo) {
    for (unsigned spins = 0;; ++spins) {
        bool ok = true;
        if (active) ok = (__hip_atomic_load(flag, RLX_AGENT) == tag);
        if (__all(ok)) break;
        if (spins > SPIN_CAP) { __hip_atomic_store(tmo, 1u, RLX_AGENT); break; }
        __builtin_amdgcn_s_sleep(2);
    }
    asm volatile("" ::: "memory");
}
__device__ __forceinline__ void sbar(lptr lds, unsigned& epoch, int lane) {
    asm volatile("s_waitcnt lgkmcnt(0)" ::: "memory");
    epoch += 4;
    LAS unsigned* cnt = (LAS unsigned*)(lds + L_CNT);
    if (lane == 0) __hip_atomic_fetch_add(cnt, 1u, __ATOMIC_RELAXED, __HIP_MEMORY_SCOPE_WORKGROUP);
    for (unsigned spins = 0; spins < (1u << 24); ++spins) {
        const unsigned v = __builtin_amdgcn_readfirstlane(__hip_atomic_load(cnt, __ATOMIC_RELAXED, __HIP_MEMORY_SCOPE_WORKGROUP));
        if (v >= epoch) break;
        __builtin_amdgcn_s_sleep(1);
    }
    asm volatile("" ::: "memory");
}

struct cpx { float re, im; };
__device__ __forceinline__ cpx cmul(cpx a, cpx b) { return {a.re * b.re - a.im * b.im, a.re * b.im + a.im * b.re}; }
__device__ __forceinline__ cpx cfma(cpx a, cpx b, cpx c) { return {__builtin_fmaf(a.re, b.re, __builtin_fmaf(-a.im, b.im, c.re)), __builtin_fmaf(a.re, b.im, __builtin_fmaf(a.im, b.re, c.im))}; }

__global__ __launch_bounds__(512, 2) void fused_kernel(
    const u16* __restrict__ xA, const u16* __restrict__ LamRe, const u16* __restrict__ LamIm, const u16* __restrict__ Bre, const u16* __restrict__ Bim,
    const u16* __restrict__ Cre, const u16* __restrict__ Cim, const u16* __restrict__ logdt, const u16* __restrict__ Dskip,
    const u16* __restrict__ xB, const u16* __restrict__ Wqkv, const u16* __restrict__ Wo, float* __restrict__ out, char* __restrict__ ws)
{
    extern __shared__ __attribute__((aligned(16))) char lds_raw[];
    const lptr lds = (lptr)lds_raw;
    const unsigned ldsb = (unsigned)(uintptr_t)lds_raw;
    const int tid = threadIdx.x, lane = tid & 63, r32 = lane & 31, hi = lane >> 5;
    const int wave = __builtin_amdgcn_readfirstlane(tid >> 6);
    const int b = blockIdx.x, xj = b & 7, idx = b >> 3;
    const int head = 8 * (xj & 1) + (idx & 7), rb = 4 * (xj >> 1) + (idx >> 3);
    GAS u64* const flags = (GAS u64*)(ws + WS_FLAG);
    GAS unsigned* const tmo = (GAS unsigned*)(ws + WS_TMO);
    const __amdgpu_buffer_rsrc_t wsr = __builtin_amdgcn_make_buffer_rsrc(ws, 0, 16u << 20, 0x00020000);

    u64 tag;
    { u64 z = __hip_atomic_load((GAS u64*)(ws + WS_EP), RLX_AGENT) + 0x9E3779B97F4A7C15ull; z = (z ^ (z >> 30)) * 0xBF58476D1CE4E5B9ull; z = (z ^ (z >> 27)) * 0x94D049BB133111EBull; tag = z ^ (z >> 31); }
    if (tid == 0) *(LAS unsigned*)(lds + L_CNT) = 0;

    const int tw = wave & 3;
    f32x16 acc0 = {}, acc1 = {};
    {
        const i32x4 srdX = make_srd(xB, SEQ * DM * 2), srdW = make_srd(Wqkv, DM * 3 * DM * 2);
        unsigned vx[2], vw[2];
#pragma unroll
        for (int i = 0; i < 2; ++i) { const int row = 8 * (2 * wave + i) + (lane >> 3); vx[i] = (unsigned)((128 * rb + row) * 896 + (((lane & 7) ^ ((row >> 1) & 7)) << 4)); }
#pragma unroll
        for (int i = 0; i < 2; ++i) { const int q = wave < 4 ? 2 * wave + i : 8 + (wave - 4); vw[i] = (unsigned)((16 * (q & 3) + (lane >> 2)) * 2688 + (q >> 2) * 896 + head * 56 + ((lane & 3) << 4)); }
        auto issue = [&](int c, int slot) {
            const unsigned sb = ldsb + slot * L_SLOT;
            dma16(srdX, vx[0], c * 128, sb + (2 * wave) * 1024);
            dma16(srdX, vx[1], c * 128, sb + (2 * wave + 1) * 1024);
            if (wave < 4) { dma16(srdW, vw[0], c * 64 * 2688, sb + 16384 + (2 * wave) * 1024); dma16(srdW, vw[1], c * 64 * 2688, sb + 16384 + (2 * wave + 1) * 1024); }
            else dma16(srdW, vw[0], c * 64 * 2688, sb + 16384 + (8 + wave - 4) * 1024);
        };
        const int xrow = 32 * tw + r32;
        const unsigned xoff = xrow * 128, xsw = (xrow >> 1) & 7;
        const unsigned woff = 16384 + (8 * hi + ((lane & 15) >> 2)) * 64 + (16 * ((lane >> 4) & 1) + 4 * (lane & 3)) * 2;
        auto compute = [&](int slot) {
            const lptr sb = lds + slot * L_SLOT;
#pragma unroll
            for (int s = 0; s < 4; ++s) {
                const bf16x8 xf = lds_b128(sb + xoff + (((2 * s + hi) ^ xsw) << 4));
                if (wave < 4) {
                    const bf16x8 wq = lds_tr2(sb + woff + s * 1024, sb + woff + s * 1024 + 256);
                    const bf16x8 wk = lds_tr2(sb + woff + 4096 + s * 1024, sb + woff + 4096 + s * 1024 + 256);
                    acc0 = MFMA(wq, xf, acc0); acc1 = MFMA(wk, xf, acc1);
                } else {
                    const bf16x8 wv = lds_tr2(sb + woff + 8192 + s * 1024, sb + woff + 8192 + s * 1024 + 256);
                    acc0 = MFMA(xf, wv, acc0);
                }
            }
        };
        issue(0, 0); issue(1, 1); issue(2, 2); issue(3, 3); issue(4, 4);
        if (wave < 4) {
            WAIT_BAR(16); compute(0);
            WAIT_BAR(12); issue(5, 0); compute(1);
            WAIT_BAR(12); issue(6, 1); compute(2);
            WAIT_BAR(12); compute(3);
            WAIT_BAR(8); compute(4);
            WAIT_BAR(4); compute(0);
            WAIT_BAR(0); compute(1);
        } else {
            WAIT_BAR(12); compute(0);
            WAIT_BAR(9); issue(5, 0); compute(1);
            WAIT_BAR(9); issue(6, 1); compute(2);
            WAIT_BAR(9); compute(3);
            WAIT_BAR(6); compute(4);
            WAIT_BAR(3); compute(0);
            WAIT_BAR(0); compute(1);
        }
    }
    bf16x8 kown0, kown1;
    {
        const unsigned kvoff = (unsigned)(WS_KV + ((head * 64 + 4 * rb + tw) * 4) * 1024 + lane * 16);
        if (wave < 4) {
            constexpr float QS = 0.18898223650461361f * 1.4426950408889634f;
#pragma unroll
            for (int r = 0; r < 16; ++r) acc0[r] *= QS;
            if (hi) { acc0[12] = 0.f; acc0[13] = 0.f; acc0[14] = 0.f; acc0[15] = 0.f; acc1[12] = 1.f; acc1[13] = 1.f; acc1[14] = 0.f; acc1[15] = 0.f; }
            *(LAS bf16x8*)(lds + L_Q + (tw * 2 + 0) * 1024 + lane * 16) = pack8(acc0, 0);
            *(LAS bf16x8*)(lds + L_Q + (tw * 2 + 1) * 1024 + lane * 16) = pack8(acc0, 1);
            kown0 = pack8(acc1, 0); kown1 = pack8(acc1, 1);
            st16_sc1(wsr, kvoff, __builtin_bit_cast(u32x4, kown0)); st16_sc1(wsr, kvoff + 1024, __builtin_bit_cast(u32x4, kown1));
        } else {
            if (r32 >= 28) {
                const float f = r32 == 28 ? 1.f : 0.f;
#pragma unroll
                for (int r = 0; r < 16; ++r) acc0[r] = f;
            }
            st16_sc1(wsr, kvoff + 2048, __builtin_bit_cast(u32x4, pack8(acc0, 0))); st16_sc1(wsr, kvoff + 3072, __builtin_bit_cast(u32x4, pack8(acc0, 1)));
        }
        WAIT_BAR(0);
        if (tid == 0) __hip_atomic_store(flags + 16 * (F_KV + head * 16 + rb), tag, RLX_AGENT);
    }

    unsigned sepoch = 0;
    f32x16 O[4];
    float meff[4];
    if (wave < 4) {
        bf16x8 Q[4][2];
#pragma unroll
        for (int qt = 0; qt < 4; ++qt) { Q[qt][0] = lds_b128(lds + L_Q + (qt * 2) * 1024 + lane * 16); Q[qt][1] = lds_b128(lds + L_Q + (qt * 2 + 1) * 1024 + lane * 16); }
        const f32x16 zero = {};
#pragma unroll
        for (int qt = 0; qt < 4; ++qt) {
            f32x16 s = MFMA(kown0, Q[qt][0], zero); s = MFMA(kown1, Q[qt][1], s);
            float m = s[0];
#pragma unroll
            for (int r = 1; r < 16; ++r) m = __builtin_fmaxf(m, s[r]);
            { auto rr = __builtin_amdgcn_permlane32_swap(__float_as_uint(m), __float_as_uint(m), false, false); m = __builtin_fmaxf(__uint_as_float(rr[0]), __uint_as_float(rr[1])); }
            const unsigned p1 = cvtpk(m, 0.f); const float mh = __uint_as_float(p1 << 16);
            const unsigned p2 = cvtpk(m - mh, 0.f); const float ml = __uint_as_float(p2 << 16);
            meff[qt] = mh + ml;
            u32x4 q1 = __builtin_bit_cast(u32x4, Q[qt][1]);
            if (hi) q1[2] = ((p1 & 0xffffu) | (p2 << 16)) ^ 0x80008000u;
            Q[qt][1] = __builtin_bit_cast(bf16x8, q1);
            O[qt] = zero;
        }
        wait_flags(flags + 16 * (F_KV + head * 16 + (lane & 15)), lane < 16, tag, tmo);
        const unsigned kvbase = (unsigned)(WS_KV + (head * 64 + wave) * 4096 + lane * 16);
        for (int j = 0; j < 16; ++j) {
            const unsigned off = kvbase + ((rb + j) & 15) * 16384;
            const bf16x8 k0 = __builtin_bit_cast(bf16x8, ld16_sc1(wsr, off)), k1 = __builtin_bit_cast(bf16x8, ld16_sc1(wsr, off + 1024));
            const bf16x8 v0 = __builtin_bit_cast(bf16x8, ld16_sc1(wsr, off + 2048)), v1 = __builtin_bit_cast(bf16x8, ld16_sc1(wsr, off + 3072));
#pragma unroll
            for (int qt = 0; qt < 4; ++qt) {
                f32x16 s = MFMA(k0, Q[qt][0], zero); s = MFMA(k1, Q[qt][1], s);
#pragma unroll
                for (int r = 0; r < 16; ++r) s[r] = __builtin_amdgcn_exp2f(s[r]);
                O[qt] = MFMA(v0, pack8(s, 0), O[qt]); O[qt] = MFMA(v1, pack8(s, 1), O[qt]);
            }
        }
        bool bad = false;
#pragma unroll
        for (int qt = 0; qt < 4; ++qt) bad |= hi && !(O[qt][12] < 1.0995116e12f);
        if (__any(bad)) {
#pragma unroll
            for (int qt = 0; qt < 4; ++qt) {
                u32x4 q1 = __builtin_bit_cast(u32x4, Q[qt][1]); if (hi) q1[2] = 0; Q[qt][1] = __builtin_bit_cast(bf16x8, q1);
                O[qt] = zero; meff[qt] = -3.0e38f;
            }
            for (int j = 0; j < 16; ++j) {
                const unsigned off = kvbase + ((rb + j) & 15) * 16384;
                const bf16x8 k0 = __builtin_bit_cast(bf16x8, ld16_sc1(wsr, off)), k1 = __builtin_bit_cast(bf16x8, ld16_sc1(wsr, off + 1024));
                const bf16x8 v0 = __builtin_bit_cast(bf16x8, ld16_sc1(wsr, off + 2048)), v1 = __builtin_bit_cast(bf16x8, ld16_sc1(wsr, off + 3072));
#pragma unroll
                for (int qt = 0; qt < 4; ++qt) {
                    f32x16 s = MFMA(k0, Q[qt][0], zero); s = MFMA(k1, Q[qt][1], s);
                    float m = s[0];
#pragma unroll
                    for (int r = 1; r < 16; ++r) m = __builtin_fmaxf(m, s[r]);
                    { auto rr = __builtin_amdgcn_permlane32_swap(__float_as_uint(m), __float_as_uint(m), false, false); m = __builtin_fmaxf(__uint_as_float(rr[0]), __uint_as_float(rr[1])); }
                    const float mn = __builtin_fmaxf(meff[qt], m), al = __builtin_amdgcn_exp2f(meff[qt] - mn);
                    meff[qt] = mn;
#pragma unroll
                    for (int r = 0; r < 16; ++r) { s[r] = __builtin_amdgcn_exp2f(s[r] - mn); O[qt][r] *= al; }
                    O[qt] = MFMA(v0, pack8(s, 0), O[qt]); O[qt] = MFMA(v1, pack8(s, 1), O[qt]);
                }
            }
        }
        if (!hi) {
#pragma unroll
            for (int qt = 0; qt < 4; ++qt) *(LAS float*)(lds + L_M + ((wave * 4 + qt) * 32 + r32) * 4) = meff[qt];
        }
    } else if (b < 224) {
        __builtin_amdgcn_s_setprio(2);
        const int sw = wave - 4;
        const int c = 2 * xj + (idx & 1), g = idx >> 1;
        cpx lam, coef;
        {
            const int p = 32 * g + r32;
            const float lr = bf2f(LamRe[p]), li = bf2f(LamIm[p]);
            const float dtf = __expf(bf2f(logdt[p]));
            const float dt = __uint_as_float(cvtpk(dtf, 0.f) << 16);
            const float a = dt * lr, bb = dt * li;
            const float ea = __expf(a);
            float rev = bb * 0.15915494309189535f; rev -= __builtin_floorf(rev);
            lam.re = ea * __builtin_amdgcn_cosf(rev); lam.im = ea * __builtin_amdgcn_sinf(rev);
            const float nr = lam.re - 1.f, ni = lam.im, den = 1.f / (lr * lr + li * li);
            coef.re = (nr * lr + ni * li) * den; coef.im = (ni * lr - nr * li) * den;
        }
        f32x16 ur = {}, ui = {};
        {
            const i32x4 srdX = make_srd(xA, SEQ * DM * 2), srdR = make_srd(Bre, DM * DM * 2), srdI = make_srd(Bim, DM * DM * 2);
            unsigned vx[4];
#pragma unroll
            for (int i = 0; i < 4; ++i) { const int row = 8 * (4 * sw + i) + (lane >> 3); vx[i] = (unsigned)((128 * c + row) * 896 + (((lane & 7) ^ ((row >> 1) & 7)) << 4)); }
            const int brow = 8 * sw + (lane >> 3);
            const unsigned vb = (unsigned)((32 * g + brow) * 896 + (((lane & 7) ^ ((brow >> 1) & 7)) << 4));
            auto issue = [&](int ck) {
                const unsigned sb = ldsb + (ck & 3) * 24576;
#pragma unroll
                for (int i = 0; i < 4; ++i) dma16(srdX, vx[i], ck * 128, sb + (4 * sw + i) * 1024);
                dma16(srdR, vb, ck * 128, sb + 16384 + sw * 1024);
                dma16(srdI, vb, ck * 128, sb + 20480 + sw * 1024);
            };
            const int trow = 32 * sw + 16 * ((r32 >> 2) & 1) + (r32 & 3) + 4 * (r32 >> 3);
            const unsigned aoff = trow * 128, asw = (trow >> 1) & 7, boff = 16384 + r32 * 128, bsw = (r32 >> 1) & 7;
            auto compute = [&](int ck) {
                const lptr sb = lds + (ck & 3) * 24576;
#pragma unroll
                for (int s = 0; s < 4; ++s) {
                    const bf16x8 xf = lds_b128(sb + aoff + (((2 * s + hi) ^ asw) << 4));
                    const bf16x8 br = lds_b128(sb + boff + (((2 * s + hi) ^ bsw) << 4));
                    const bf16x8 bi = lds_b128(sb + boff + 4096 + (((2 * s + hi) ^ bsw) << 4));
                    ur = MFMA(xf, br, ur); ui = MFMA(xf, bi, ui);
                }
            };
            issue(0); issue(1); issue(2);
            asm volatile("s_waitcnt vmcnt(12)" ::: "memory"); sbar(lds, sepoch, lane); issue(3); compute(0);
            asm volatile("s_waitcnt vmcnt(12)" ::: "memory"); sbar(lds, sepoch, lane); issue(4); compute(1);
            asm volatile("s_waitcnt vmcnt(12)" ::: "memory"); sbar(lds, sepoch, lane); issue(5); compute(2);
            asm volatile("s_waitcnt vmcnt(12)" ::: "memory"); sbar(lds, sepoch, lane); issue(6); compute(3);
            asm volatile("s_waitcnt vmcnt(12)" ::: "memory"); sbar(lds, sepoch, lane); compute(4);
            asm volatile("s_waitcnt vmcnt(6)" ::: "memory"); sbar(lds, sepoch, lane); compute(5);
            asm volatile("s_waitcnt vmcnt(0)" ::: "memory"); sbar(lds, sepoch, lane); compute(6);
        }
        cpx e = {0.f, 0.f};
#pragma unroll
        for (int i = 0; i < 16; ++i) {
            const float u = ur[i], v = ui[i];
            ur[i] = coef.re * u - coef.im * v; ui[i] = coef.re * v + coef.im * u;
            e = cfma(lam, e, (cpx){ur[i], ui[i]});
        }
        const int seg = 2 * sw + hi;
        *(LAS f32x2*)(lds + L_SEG + (seg * 32 + r32) * 8) = (f32x2){e.re, e.im};
        sbar(lds, sepoch, lane);
        const int tb = 4 * xj + (idx & 3), db = idx >> 2;
        {
            const i32x4 srdR = make_srd(Cre, DM * DM * 2), srdI = make_srd(Cim, DM * DM * 2);
            const int row = 16 * sw + (lane >> 2);
            const unsigned vc = (unsigned)((64 * db + row) * 896 + (((lane & 3) ^ ((row >> 2) & 3)) << 4));
            for (int gg = 0; gg < 14; ++gg) { dma16(srdR, vc, gg * 64, ldsb + gg * 8192 + sw * 1024); dma16(srdI, vc, gg * 64, ldsb + gg * 8192 + 4096 + sw * 1024); }
        }
        cpx l2 = cmul(lam, lam), l4 = cmul(l2, l2), l8 = cmul(l4, l4), l16 = cmul(l8, l8), l32 = cmul(l16, l16), l64 = cmul(l32, l32), l128 = cmul(l64, l64);
        cpx cseg = {0.f, 0.f}, E = {0.f, 0.f};
#pragma unroll
        for (int s2 = 0; s2 < 8; ++s2) {
            const f32x2 es = *(const LAS f32x2*)(lds + L_SEG + (s2 * 32 + r32) * 8);
            E = cfma(l16, E, (cpx){es[0], es[1]});
            if (s2 + 1 == seg) cseg = E;
        }
        if (sw == 0) {
            if (!hi) __hip_atomic_store((GAS u64*)(ws + WS_E + ((c * 14 + g) * 32 + r32) * 8), ((u64)__float_as_uint(E.im) << 32) | __float_as_uint(E.re), RLX_AGENT);
            WAIT_VM0();
            if (lane == 0) __hip_atomic_store(flags + 16 * (F_E + c * 16 + g), tag, RLX_AGENT);
        }
        wait_flags(flags + 16 * (F_E + (lane & 15) * 16 + g), lane < c, tag, tmo);
        cpx carry = {0.f, 0.f};
        for (int c2 = 0; c2 < c; ++c2) {
            const u64 v = __hip_atomic_load((GAS u64*)(ws + WS_E + ((c2 * 14 + g) * 32 + r32) * 8), RLX_AGENT);
            carry = cfma(l128, carry, (cpx){__uint_as_float((unsigned)v), __uint_as_float((unsigned)(v >> 32))});
        }
        cpx pw = {1.f, 0.f};
        if (seg & 1) pw = l16;
        if (seg & 2) pw = cmul(pw, l32);
        if (seg & 4) pw = cmul(pw, l64);
        cpx h = cfma(pw, carry, cseg);
#pragma unroll
        for (int i = 0; i < 16; ++i) { h = cfma(lam, h, (cpx){ur[i], ui[i]}); ur[i] = 2.f * h.re; ui[i] = -2.f * h.im; }
        {
            const lptr hb = lds + L_HT + sw * 4096;
            *(LAS bf16x8*)(hb + r32 * 64 + hi * 32) = pack8(ur, 0); *(LAS bf16x8*)(hb + r32 * 64 + hi * 32 + 16) = pack8(ur, 1);
            *(LAS bf16x8*)(hb + 2048 + r32 * 64 + hi * 32) = pack8(ui, 0); *(LAS bf16x8*)(hb + 2048 + r32 * 64 + hi * 32 + 16) = pack8(ui, 1);
            asm volatile("s_waitcnt lgkmcnt(0)" ::: "memory");
            const lptr tp = hb + (8 * hi + ((lane & 15) >> 2)) * 64 + (16 * ((lane >> 4) & 1) + 4 * (lane & 3)) * 2;
            const unsigned hoff = (unsigned)(WS_HF + ((4 * c + sw) * 56 + 4 * g) * 1024 + lane * 16);
#pragma unroll
            for (int f = 0; f < 4; ++f) st16_sc1(wsr, hoff + f * 1024, __builtin_bit_cast(u32x4, lds_tr2(tp + f * 1024, tp + f * 1024 + 256)));
            WAIT_VM0();
            sbar(lds, sepoch, lane);
            if (sw == 0 && lane == 0) __hip_atomic_store(flags + 16 * (F_H + c * 16 + g), tag, RLX_AGENT);
        }
        {
            const int tt = 2 * tb + (sw & 1), kh = sw >> 1;
            wait_flags(flags + 16 * (F_H + (tb >> 1) * 16 + (lane & 15)), lane < 14, tag, tmo);
            f32x16 y0 = {}, y1 = {};
            const unsigned hoff = (unsigned)(WS_HF + (tt * 56 + 28 * kh) * 1024 + lane * 16);
            const unsigned coff = r32 * 64, csw = (r32 >> 2) & 3;
#pragma unroll
            for (int gi = 0; gi < 7; ++gi) {
                const lptr cb = lds + (7 * kh + gi) * 8192;
#pragma unroll
                for (int f = 0; f < 4; ++f) {
                    const bf16x8 a = __builtin_bit_cast(bf16x8, ld16_sc1(wsr, hoff + (4 * gi + f) * 1024));
                    const lptr cp = cb + (f >> 1) * 4096 + coff + (((2 * (f & 1) + hi) ^ csw) << 4);
                    y0 = MFMA(a, lds_b128(cp), y0); y1 = MFMA(a, lds_b128(cp + 2048), y1);
                }
            }
            const lptr rbuf = lds + L_HT;
            const f32x16& snd = kh ? y0 : y1;
#pragma unroll
            for (int q4 = 0; q4 < 4; ++q4) *(LAS f32x4*)(rbuf + (sw * 4 + q4) * 1024 + lane * 16) = (f32x4){snd[4 * q4], snd[4 * q4 + 1], snd[4 * q4 + 2], snd[4 * q4 + 3]};
            sbar(lds, sepoch, lane);
            f32x16 y = kh ? y1 : y0;
#pragma unroll
            for (int q4 = 0; q4 < 4; ++q4) { const f32x4 o4 = *(const LAS f32x4*)(rbuf + ((sw ^ 2) * 4 + q4) * 1024 + lane * 16); y[4 * q4] += o4[0]; y[4 * q4 + 1] += o4[1]; y[4 * q4 + 2] += o4[2]; y[4 * q4 + 3] += o4[3]; }
            {
                const i32x4 srdO = make_srd(Wo, DM * DM * 2);
                const int nb = db;
                for (int i = sw; i < 57; i += 4) {
                    const int row = 8 * i + (lane >> 3);
                    const unsigned vo = row < 448 ? (unsigned)(row * 896 + nb * 128 + (((lane & 7) ^ (((row >> 1) & 1) << 2)) << 4)) : 0x7fffffffu;
                    dma16(srdO, vo, 0, ldsb + L_WO + i * 1024);
                }
            }
            const int d = 64 * db + 32 * kh + r32;
            const float dsk = bf2f(Dskip[d]);
#pragma unroll
            for (int r = 0; r < 16; ++r) {
                const int t = 32 * tt + (r & 3) + 8 * (r >> 2) + 4 * hi;
                const float xd = __uint_as_float(cvtpk(bf2f(xA[t * DM + d]) * dsk, 0.f) << 16);
                out[t * OUTW + d] = y[r] + xd;
            }
        }
        __builtin_amdgcn_s_setprio(0);
    }

    WAIT_BAR(0);
    if (wave < 4) {
#pragma unroll
        for (int qt = 0; qt < 4; ++qt) {
            float M = -3.0e38f;
#pragma unroll
            for (int w2 = 0; w2 < 4; ++w2) M = __builtin_fmaxf(M, *(const LAS float*)(lds + L_M + ((w2 * 4 + qt) * 32 + r32) * 4));
            const float f = __builtin_amdgcn_exp2f(meff[qt] - M);
#pragma unroll
            for (int q4 = 0; q4 < 4; ++q4)
                *(LAS f32x4*)(lds + ((wave * 4 + qt) * 4 + q4) * 1024 + lane * 16) = (f32x4){O[qt][4 * q4] * f, O[qt][4 * q4 + 1] * f, O[qt][4 * q4 + 2] * f, O[qt][4 * q4 + 3] * f};
        }
    }
    WAIT_BAR(0);
    {
        const int qt = wave & 3, s = wave >> 2;
        f32x16 a = {}; float l = 0.f;
#pragma unroll
        for (int w2 = 0; w2 < 4; ++w2) {
            const lptr pb = lds + ((w2 * 4 + qt) * 4) * 1024;
            const f32x4 x0 = *(const LAS f32x4*)(pb + (2 * s) * 1024 + lane * 16), x1 = *(const LAS f32x4*)(pb + (2 * s + 1) * 1024 + lane * 16);
            a[0] += x0[0]; a[1] += x0[1]; a[2] += x0[2]; a[3] += x0[3]; a[4] += x1[0]; a[5] += x1[1]; a[6] += x1[2]; a[7] += x1[3];
            l += *(const LAS float*)(pb + 3 * 1024 + (32 + r32) * 16);
        }
        const float inv = 1.f / l;
#pragma unroll
        for (int r = 0; r < 8; ++r) a[r] *= inv;
        if (s == 1 && hi) { a[4] = 0.f; a[5] = 0.f; a[6] = 0.f; a[7] = 0.f; }
        st16_sc1(wsr, (unsigned)(WS_OF + (((4 * rb + qt) * 16 + head) * 2 + s) * 1024 + lane * 16), __builtin_bit_cast(u32x4, pack8(a, 0)));
    }
    WAIT_BAR(0);
    if (tid == 0) __hip_atomic_store(flags + 16 * (F_O + head * 16 + rb), tag, RLX_AGENT);

    if (b < 224) {
        const int tb = 4 * xj + (idx & 3), nb = idx >> 2;
        f32x16 y0 = {}, y1 = {};
        const int tt = 2 * tb + (wave & 1), kh = (wave >> 1) & 1;
        if (wave < 4) {
            wait_flags(flags + 16 * (F_O + (lane & 15) * 16 + (tb >> 1)), lane < 16, tag, tmo);
            const unsigned ooff = (unsigned)(WS_OF + (tt * 32 + 16 * kh) * 1024 + lane * 16);
            const int q = (lane & 15) >> 2, p = lane & 3, blk = (lane >> 4) & 1;
#pragma unroll
            for (int f = 0; f < 16; ++f) {
                const bf16x8 a = __builtin_bit_cast(bf16x8, ld16_sc1(wsr, ooff + f * 1024));
                const int k0 = (8 * kh + (f >> 1)) * 28 + 16 * (f & 1) + 4 * hi + q;
                const int k1 = k0 + 8;
                const int c16 = 2 * blk + (p >> 1);
                const lptr wb = lds + L_WO + 8 * (p & 1);
                const lptr a0 = wb + k0 * 128, a1 = wb + k1 * 128;
                const int s0 = ((k0 >> 1) & 1) << 2, s1 = ((k1 >> 1) & 1) << 2;
                y0 = MFMA(a, lds_tr2(a0 + ((c16 ^ s0) << 4), a1 + ((c16 ^ s1) << 4)), y0);
                y1 = MFMA(a, lds_tr2(a0 + (((c16 + 4) ^ s0) << 4), a1 + (((c16 + 4) ^ s1) << 4)), y1);
            }
            const lptr rbuf = lds + L_HT;
            const f32x16& snd = kh ? y0 : y1;
#pragma unroll
            for (int q4 = 0; q4 < 4; ++q4) *(LAS f32x4*)(rbuf + (wave * 4 + q4) * 1024 + lane * 16) = (f32x4){snd[4 * q4], snd[4 * q4 + 1], snd[4 * q4 + 2], snd[4 * q4 + 3]};
        }
        WAIT_BAR(0);
        if (wave < 4) {
            const lptr rbuf = lds + L_HT;
            f32x16 y = kh ? y1 : y0;
#pragma unroll
            for (int q4 = 0; q4 < 4; ++q4) { const f32x4 o4 = *(const LAS f32x4*)(rbuf + ((wave ^ 2) * 4 + q4) * 1024 + lane * 16); y[4 * q4] += o4[0]; y[4 * q4 + 1] += o4[1]; y[4 * q4 + 2] += o4[2]; y[4 * q4 + 3] += o4[3]; }
            const int n = 448 + 64 * nb + 32 * kh + r32;
#pragma unroll
            for (int r = 0; r < 16; ++r) {
                const int t = 32 * tt + (r & 3) + 8 * (r >> 2) + 4 * hi;
                out[t * OUTW + n] = __uint_as_float(cvtpk(y[r], 0.f) << 16);
            }
        }
    }
    if (b == 0 && wave == 7) {
        for (int i = 0; i < 4; ++i) wait_flags(flags + 16 * (F_O + i * 64 + lane), true, tag, tmo);
        if (lane == 0) __hip_atomic_store((GAS u64*)(ws + WS_EP), tag, RLX_AGENT);
    }
}

extern "C" void kernel_launch(void* const* d_in, const int* in_sizes, int n_in, void* d_out, int out_size, void* d_ws, size_t ws_size, hipStream_t stream) {
    (void)in_sizes; (void)n_in; (void)out_size; (void)ws_size;
    hipFuncSetAttribute((const void*)fused_kernel, hipFuncAttributeMaxDynamicSharedMemorySize, LDS_BYTES);
    fused_kernel<<<256, 512, LDS_BYTES, stream>>>(
        (const u16*)d_in[0], (const u16*)d_in[1], (const u16*)d_in[2], (const u16*)d_in[3], (const u16*)d_in[4], (const u16*)d_in[5], (const u16*)d_in[6],
        (const u16*)d_in[7], (const u16*)d_in[8], (const u16*)d_in[9], (const u16*)d_in[10], (const u16*)d_in[11], (float*)d_out, (char*)d_ws);
}
```

```cpp
#include <hip/hip_runtime.h>
#include <stdint.h>

typedef short bf16x8 __attribute__((ext_vector_type(8)));
typedef short s16x4 __attribute__((ext_vector_type(4)));
typedef float f32x16 __attribute__((ext_vector_type(16)));
typedef float f32x4 __attribute__((ext_vector_type(4)));
typedef float f32x2 __attribute__((ext_vector_type(2)));
typedef unsigned u32x4 __attribute__((ext_vector_type(4)));
typedef unsigned u32x2 __attribute__((ext_vector_type(2)));
typedef unsigned u32x8 __attribute__((ext_vector_type(8)));
typedef int i32x4 __attribute__((ext_vector_type(4)));
typedef unsigned long long u64;
typedef unsigned short u16;
typedef __bf16 bf16x2_t __attribute__((ext_vector_type(2)));
typedef short v4i16_t __attribute__((ext_vector_type(4)));
#if defined(__HIP_DEVICE_COMPILE__)
#define NOPK __attribute__((target("no-packed-fp32-ops")))
#else
#define NOPK
#endif
#define DEV __device__ __forceinline__ NOPK
#define F2U(x) __builtin_bit_cast(unsigned, (float)(x))
#define U2F(x) __builtin_bit_cast(float, (unsigned)(x))
#define GAS __attribute__((address_space(1)))
#define LAS __attribute__((address_space(3)))
typedef LAS char* lptr;

constexpr int SEQ = 2048, DM = 448, OUTW = 896;

constexpr unsigned WS_EP = 0, WS_TMO = 256;
constexpr unsigned WS_FLAG = 4096;
constexpr int F_KV = 0, F_H = 64, F_O = 80;
constexpr unsigned WS_KV = 1u << 20;
constexpr unsigned WS_E = 5u << 20;
constexpr unsigned WS_HF = 6u << 20;
constexpr unsigned WS_OF = 10u << 20;
constexpr unsigned WS_QF = 12u << 20;
constexpr unsigned WS_BYTES = 16u << 20;

constexpr int L_SLOT = 32768;
constexpr int L_WO = 0;
constexpr int L_XCH = 65536;
constexpr int L_QX = 148480;
constexpr int L_HT = 114688;
constexpr int L_CMB = 131072;
constexpr int L_LM = 155648;
constexpr int L_MISC = 159744;
constexpr int L_SEG = L_MISC, L_BARS = L_MISC + 2048, L_BARA = L_MISC + 2560, L_ACNT = L_MISC + 3072;
constexpr int LDS_BYTES = 163840;

constexpr unsigned CODE_BEFORE = 14080, CODE_BYTES = 31744;
#define MFMA(a, b, c) __builtin_amdgcn_mfma_f32_32x32x16_bf16(a, b, c, 0, 0, 0)
#define RLX_AGENT __ATOMIC_RELAXED, __HIP_MEMORY_SCOPE_AGENT
#define SCHED_FENCE() __builtin_amdgcn_sched_barrier(0)

DEV unsigned cvtpk(float lo, float hi) { const f32x2 v = {lo, hi}; return __builtin_bit_cast(unsigned, __builtin_convertvector(v, bf16x2_t)); }
DEV float bf2f(u16 b) { return U2F((unsigned)b << 16); }
DEV float rbf(float x) { return U2F(cvtpk(x, 0.f) << 16); }
DEV i32x4 make_srd(const void* p, unsigned bytes) {
    const u64 a = (u64)p; i32x4 r; r[0] = (int)(unsigned)a; r[1] = (int)((unsigned)(a >> 32) & 0xffffu); r[2] = (int)bytes; r[3] = 0x00020000; return r;
}
DEV void dma16(i32x4 srd, unsigned voff, unsigned soff, unsigned ldsaddr) {
    unsigned keep;
    asm volatile("s_mov_b32 %0, m0\n\ts_mov_b32 m0, %4\n\ts_nop 0\n\tbuffer_load_dwordx4 %1, %2, %3 offen lds\n\ts_mov_b32 m0, %0"
                 : "=&s"(keep) : "v"(voff), "s"(srd), "s"(soff), "s"(ldsaddr) : "memory");
}
DEV bf16x8 lds_b128(lptr p) { return *(const LAS bf16x8*)p; }
DEV s16x4 lds_tr(lptr p) { return __builtin_bit_cast(s16x4, __builtin_amdgcn_ds_read_tr16_b64_v4i16((LAS v4i16_t*)p)); }
DEV bf16x8 lds_tr2(lptr p0, lptr p1) { const s16x4 a = lds_tr(p0), b = lds_tr(p1); return (bf16x8){a[0], a[1], a[2], a[3], b[0], b[1], b[2], b[3]}; }
DEV bf16x8 pack8(const f32x16& a, int s) {
    u32x4 r; r[0] = cvtpk(a[8 * s + 0], a[8 * s + 1]); r[1] = cvtpk(a[8 * s + 2], a[8 * s + 3]); r[2] = cvtpk(a[8 * s + 4], a[8 * s + 5]); r[3] = cvtpk(a[8 * s + 6], a[8 * s + 7]);
    return __builtin_bit_cast(bf16x8, r);
}
DEV void st16_sc1(__amdgpu_buffer_rsrc_t rs, unsigned off, u32x4 v) { __builtin_amdgcn_raw_buffer_store_b128(v, rs, off, 0, 16); }
DEV u32x4 ld16_sc1(__amdgpu_buffer_rsrc_t rs, unsigned off, unsigned soff = 0) { return __builtin_amdgcn_raw_buffer_load_b128(rs, off, soff, 16); }
#define UNIFORM_BRANCH() asm volatile("")

#define WAIT_BAR(N) asm volatile("s_waitcnt vmcnt(" #N ") lgkmcnt(0)\n\ts_barrier" ::: "memory")
#define WAIT_VM(N) asm volatile("s_waitcnt vmcnt(" #N ")" ::: "memory")
#define BAR_LGKM() asm volatile("s_waitcnt lgkmcnt(0)\n\ts_barrier" ::: "memory")

constexpr unsigned SPIN_CAP = 1u << 18;
template <int PRIO>
DEV void group_wait(GAS u64* p, bool need, u64 tag, GAS unsigned* tmo, bool leader, lptr ldsword) {
    if (leader) {
#pragma unroll 1
        for (unsigned spins = 0;; ++spins) {
            const bool ok = (__hip_atomic_load(p, RLX_AGENT) == tag) | !need;
            if (__builtin_amdgcn_ballot_w64(!ok) == 0) break;
            if (spins > SPIN_CAP) { __hip_atomic_store(tmo, 1u, RLX_AGENT); break; }
            __builtin_amdgcn_s_sleep(1);
        }
        __hip_atomic_store((LAS unsigned*)ldsword, 1u, __ATOMIC_RELAXED, __HIP_MEMORY_SCOPE_WORKGROUP);
    } else {
        __builtin_amdgcn_s_setprio(0);
        while (__hip_atomic_load((LAS unsigned*)ldsword, __ATOMIC_RELAXED, __HIP_MEMORY_SCOPE_WORKGROUP) == 0) __builtin_amdgcn_s_sleep(PRIO ? 4 : 1);
    }
    __builtin_amdgcn_s_setprio(PRIO);
    asm volatile("" ::: "memory");
}
template <int PRIO>
DEV void sbar(unsigned cntw, unsigned cntr, unsigned& epoch) {
    epoch += 1;
    unsigned v;
    asm volatile("s_waitcnt lgkmcnt(0)\n\tv_mov_b32 %0, %3\n\tds_write_b32 %1, %0\n\ts_setprio 0\n"
                 "1:\n\tds_read_b32 %0, %2\n\ts_waitcnt lgkmcnt(0)\n\tv_cmp_gt_u32 vcc, %3, %0\n\ts_cbranch_vccz 2f\n\ts_sleep 2\n\ts_branch 1b\n2:\n\ts_setprio %4"
                 : "=&v"(v) : "v"(cntw), "v"(cntr), "s"(epoch), "n"(PRIO) : "memory", "vcc");
}

struct cpx { float re, im; };
DEV cpx cmul(cpx a, cpx b) { return {a.re * b.re - a.im * b.im, a.re * b.im + a.im * b.re}; }
DEV cpx cfma(cpx a, cpx b, cpx c) { return {__builtin_fmaf(a.re, b.re, __builtin_fmaf(-a.im, b.im, c.re)), __builtin_fmaf(a.re, b.im, __builtin_fmaf(a.im, b.re, c.im))}; }

__global__ __launch_bounds__(512, 2) NOPK void fused_kernel(
    const u16* __restrict__ xA, const u16* __restrict__ LamRe, const u16* __restrict__ LamIm, const u16* __restrict__ Bre, const u16* __restrict__ Bim,
    const u16* __restrict__ Cre, const u16* __restrict__ Cim, const u16* __restrict__ logdt, const u16* __restrict__ Dskip,
    const u16* __restrict__ xB, const u16* __restrict__ Wqkv, const u16* __restrict__ Wo, float* __restrict__ out, char* __restrict__ ws)
{
    extern __shared__ __attribute__((aligned(16))) char lds_raw[];
    const lptr lds = (lptr)lds_raw;
    const unsigned ldsb = (unsigned)(uintptr_t)lds_raw;
    const int tid = __builtin_amdgcn_workitem_id_x(), lane = tid & 63, r32 = lane & 31, hi = lane >> 5;
    const int wave = __builtin_amdgcn_readfirstlane(tid >> 6);
    const int b = __builtin_amdgcn_workgroup_id_x(), xj = b & 7, idx = b >> 3;
    const int head = 2 * xj + (idx & 1), rb = idx >> 1;
    const int tw = wave & 3;

    asm volatile("" :: "s"(xB), "s"(Wqkv), "s"(ws), "s"(xA), "s"(Bre), "s"(Bim), "s"(LamRe), "s"(LamIm), "s"(logdt));

    const u64 ep = __hip_atomic_load((GAS u64*)(ws + WS_EP), RLX_AGENT);
    const int c = 4 * (xj >> 1) + (idx & 3), g = 7 * (xj & 1) + (idx >> 2);
    const int tb = 4 * xj + (idx & 3), db = idx >> 2;
    const int dp = idx < 24 ? 4 * (xj & 1) + (idx >> 3) : 3, tp = 8 * (xj >> 1) + (idx < 24 ? (idx & 7) : 4 * (xj & 1) + (idx - 24));
    const __amdgpu_buffer_rsrc_t wsr = __builtin_amdgcn_make_buffer_rsrc(ws, 0, WS_BYTES, 0x00020000);
    GAS u64* const flags = (GAS u64*)(ws + WS_FLAG);
    GAS unsigned* const tmo = (GAS unsigned*)(ws + WS_TMO);
    unsigned sepoch = 0, aepoch = 0;
    const unsigned aw_ = ldsb + L_BARA + tw * 128 + r32 * 4, ar_ = ldsb + L_BARA + (lane & 3) * 128;
    f32x16 O[4];
    float meff[4];
    u64 tag;
#define MAKE_TAG() do { u64 z = ep; asm volatile("" : "+v"(z)); z += 0x9E3779B97F4A7C15ull; z = (z ^ (z >> 30)) * 0xBF58476D1CE4E5B9ull; z = (z ^ (z >> 27)) * 0x94D049BB133111EBull; tag = z ^ (z >> 31); } while (0)
    if (wave < 4) {
        if (tid < 256) *(LAS unsigned*)(lds + L_BARS + tid * 4) = 0;
        if (tid < 8) *(LAS unsigned*)(lds + L_ACNT + tid * 4) = 0;
        if (b < 224) {
            const int tt1 = wave & 1, ct = wave >> 1;
            const int tile = 2 * tp + tt1;
            unsigned vo, co;
            {
                const int C = 64 * dp + 32 * ct + r32, hd = (C * 2341) >> 16, d = C - 28 * hd;
                vo = (unsigned)(WS_KV + ((hd * 64 + tile) * 4 + 2) * 1024 + (hi * 32 + d) * 16);
                const int hs = dp < 4 ? 2 * dp : 2 * dp + 1, cnt = (dp == 3 || dp == 6) ? 3 : 2, e = lane >> 4;
                co = (ct == 0 && e < cnt) ? (unsigned)(WS_KV + (((hs + e) * 64 + tile) * 4 + 2 + ((lane >> 3) & 1)) * 1024 + (((lane >> 2) & 1) * 32 + 28 + (lane & 3)) * 16) : 0x7fffffffu;
            }
            unsigned k1[2], k2[2], q1[2], q2[2]; bool spl[2];
#pragma unroll
            for (int p = 0; p < 2; ++p) {
                const int GA = 16 * dp + 8 * ct + 2 * (2 * p + hi), hA = (GA * 9363) >> 16, uA = GA - 7 * hA;
                spl[p] = uA == 6;
                const int qd = (uA + (hA & 1)) >> 1;
                k1[p] = (unsigned)(WS_KV + (hA * 64 + tile) * 4096 + qd * 512 + r32 * 16);
                q1[p] = (unsigned)(WS_QF + (hA * 64 + tile) * 2048 + qd * 512 + r32 * 16);
                k2[p] = spl[p] ? (unsigned)(WS_KV + ((hA + 1) * 64 + tile) * 4096 + r32 * 16) : 0x7fffffffu;
                q2[p] = spl[p] ? (unsigned)(WS_QF + ((hA + 1) * 64 + tile) * 2048 + r32 * 16) : 0x7fffffffu;
            }
            asm volatile("" : "+v"(vo), "+v"(co), "+v"(k1[0]), "+v"(k1[1]), "+v"(k2[0]), "+v"(k2[1]), "+v"(q1[0]), "+v"(q1[1]), "+v"(q2[0]), "+v"(q2[1]));
            f32x16 aq, ak, av;
            {
                const int xrow = 32 * tt1 + r32;
                const unsigned xoff = ldsb + xrow * 128, xsw = (xrow >> 1) & 3;
                const unsigned ax0 = xoff + (((0 + hi) ^ xsw) << 4), ax1 = xoff + (((2 + hi) ^ xsw) << 4), ax2 = xoff + (((4 + hi) ^ xsw) << 4), ax3 = xoff + (((6 + hi) ^ xsw) << 4);
                const int q = (lane & 15) >> 2, p = lane & 3, c16 = 4 * ct + 2 * ((lane >> 4) & 1) + (p >> 1), sz = ((q >> 1) & 1) << 2;
                const unsigned aw = ldsb + 8192 + (8 * hi + q) * 128 + ((c16 ^ sz) << 4) + 8 * (p & 1);
                asm volatile(
    "v_mov_b32 v144, %[ax0]\n\t"
    "v_mov_b32 v145, %[ax1]\n\t"
    "v_mov_b32 v146, %[ax2]\n\t"
    "v_mov_b32 v147, %[ax3]\n\t"
    "v_mov_b32 v148, %[aw]\n\t"
    "v_mov_b32 v64, 0\n\t"
    "v_mov_b32 v65, 0\n\t"
    "v_mov_b32 v66, 0\n\t"
    "v_mov_b32 v67, 0\n\t"
    "v_mov_b32 v68, 0\n\t"
    "v_mov_b32 v69, 0\n\t"
    "v_mov_b32 v70, 0\n\t"
    "v_mov_b32 v71, 0\n\t"
    "v_mov_b32 v72, 0\n\t"
    "v_mov_b32 v73, 0\n\t"
    "v_mov_b32 v74, 0\n\t"
    "v_mov_b32 v75, 0\n\t"
    "v_mov_b32 v76, 0\n\t"
    "v_mov_b32 v77, 0\n\t"
    "v_mov_b32 v78, 0\n\t"
    "v_mov_b32 v79, 0\n\t"
    "v_mov_b32 v80, 0\n\t"
    "v_mov_b32 v81, 0\n\t"
    "v_mov_b32 v82, 0\n\t"
    "v_mov_b32 v83, 0\n\t"
    "v_mov_b32 v84, 0\n\t"
    "v_mov_b32 v85, 0\n\t"
    "v_mov_b32 v86, 0\n\t"
    "v_mov_b32 v87, 0\n\t"
    "v_mov_b32 v88, 0\n\t"
    "v_mov_b32 v89, 0\n\t"
    "v_mov_b32 v90, 0\n\t"
    "v_mov_b32 v91, 0\n\t"
    "v_mov_b32 v92, 0\n\t"
    "v_mov_b32 v93, 0\n\t"
    "v_mov_b32 v94, 0\n\t"
    "v_mov_b32 v95, 0\n\t"
    "v_mov_b32 v96, 0\n\t"
    "v_mov_b32 v97, 0\n\t"
    "v_mov_b32 v98, 0\n\t"
    "v_mov_b32 v99, 0\n\t"
    "v_mov_b32 v100, 0\n\t"
    "v_mov_b32 v101, 0\n\t"
    "v_mov_b32 v102, 0\n\t"
    "v_mov_b32 v103, 0\n\t"
    "v_mov_b32 v104, 0\n\t"
    "v_mov_b32 v105, 0\n\t"
    "v_mov_b32 v106, 0\n\t"
    "v_mov_b32 v107, 0\n\t"
    "v_mov_b32 v108, 0\n\t"
    "v_mov_b32 v109, 0\n\t"
    "v_mov_b32 v110, 0\n\t"
    "v_mov_b32 v111, 0\n\t"
    "s_barrier\n\t"
    "s_barrier\n\t"
    "s_barrier\n\t"
    "ds_read_b128 v[112:115], v144\n\t"
    "ds_read_b64_tr_b16 v[120:121], v148 offset:0\n\t"
    "ds_read_b64_tr_b16 v[122:123], v148 offset:512\n\t"
    "ds_read_b64_tr_b16 v[128:129], v148 offset:8192\n\t"
    "ds_read_b64_tr_b16 v[130:131], v148 offset:8704\n\t"
    "ds_read_b64_tr_b16 v[136:137], v148 offset:16384\n\t"
    "ds_read_b64_tr_b16 v[138:139], v148 offset:16896\n\t"
    "ds_read_b128 v[116:119], v145\n\t"
    "ds_read_b64_tr_b16 v[124:125], v148 offset:2048\n\t"
    "ds_read_b64_tr_b16 v[126:127], v148 offset:2560\n\t"
    "ds_read_b64_tr_b16 v[132:133], v148 offset:10240\n\t"
    "ds_read_b64_tr_b16 v[134:135], v148 offset:10752\n\t"
    "ds_read_b64_tr_b16 v[140:141], v148 offset:18432\n\t"
    "ds_read_b64_tr_b16 v[142:143], v148 offset:18944\n\t"
    "s_waitcnt lgkmcnt(7)\n\t"
    "v_mfma_f32_32x32x16_bf16 v[64:79], v[120:123], v[112:115], v[64:79]\n\t"
    "v_mfma_f32_32x32x16_bf16 v[80:95], v[128:131], v[112:115], v[80:95]\n\t"
    "v_mfma_f32_32x32x16_bf16 v[96:111], v[112:115], v[136:139], v[96:111]\n\t"
    "ds_read_b128 v[112:115], v146\n\t"
    "ds_read_b64_tr_b16 v[120:121], v148 offset:4096\n\t"
    "ds_read_b64_tr_b16 v[122:123], v148 offset:4608\n\t"
    "ds_read_b64_tr_b16 v[128:129], v148 offset:12288\n\t"
    "ds_read_b64_tr_b16 v[130:131], v148 offset:12800\n\t"
    "ds_read_b64_tr_b16 v[136:137], v148 offset:20480\n\t"
    "ds_read_b64_tr_b16 v[138:139], v148 offset:20992\n\t"
    "s_waitcnt lgkmcnt(7)\n\t"
    "v_mfma_f32_32x32x16_bf16 v[64:79], v[124:127], v[116:119], v[64:79]\n\t"
    "v_mfma_f32_32x32x16_bf16 v[80:95], v[132:135], v[116:119], v[80:95]\n\t"
    "v_mfma_f32_32x32x16_bf16 v[96:111], v[116:119], v[140:143], v[96:111]\n\t"
    "ds_read_b128 v[116:119], v147\n\t"
    "ds_read_b64_tr_b16 v[124:125], v148 offset:6144\n\t"
    "ds_read_b64_tr_b16 v[126:127], v148 offset:6656\n\t"
    "ds_read_b64_tr_b16 v[132:133], v148 offset:14336\n\t"
    "ds_read_b64_tr_b16 v[134:135], v148 offset:14848\n\t"
    "ds_read_b64_tr_b16 v[140:141], v148 offset:22528\n\t"
    "ds_read_b64_tr_b16 v[142:143], v148 offset:23040\n\t"
    "s_waitcnt lgkmcnt(7)\n\t"
    "v_mfma_f32_32x32x16_bf16 v[64:79], v[120:123], v[112:115], v[64:79]\n\t"
    "v_mfma_f32_32x32x16_bf16 v[80:95], v[128:131], v[112:115], v[80:95]\n\t"
    "v_mfma_f32_32x32x16_bf16 v[96:111], v[112:115], v[136:139], v[96:111]\n\t"
    "s_waitcnt lgkmcnt(0)\n\t"
    "v_mfma_f32_32x32x16_bf16 v[64:79], v[124:127], v[116:119], v[64:79]\n\t"
    "v_mfma_f32_32x32x16_bf16 v[80:95], v[132:135], v[116:119], v[80:95]\n\t"
    "v_mfma_f32_32x32x16_bf16 v[96:111], v[116:119], v[140:143], v[96:111]\n\t"
    "v_add_u32 v144, 32768, v144\n\t"
    "v_add_u32 v145, 32768, v145\n\t"
    "v_add_u32 v146, 32768, v146\n\t"
    "v_add_u32 v147, 32768, v147\n\t"
    "v_add_u32 v148, 32768, v148\n\t"
    "s_barrier\n\t"
    "ds_read_b128 v[112:115], v144\n\t"
    "ds_read_b64_tr_b16 v[120:121], v148 offset:0\n\t"
    "ds_read_b64_tr_b16 v[122:123], v148 offset:512\n\t"
    "ds_read_b64_tr_b16 v[128:129], v148 offset:8192\n\t"
    "ds_read_b64_tr_b16 v[130:131], v148 offset:8704\n\t"
    "ds_read_b64_tr_b16 v[136:137], v148 offset:16384\n\t"
    "ds_read_b64_tr_b16 v[138:139], v148 offset:16896\n\t"
    "ds_read_b128 v[116:119], v145\n\t"
    "ds_read_b64_tr_b16 v[124:125], v148 offset:2048\n\t"
    "ds_read_b64_tr_b16 v[126:127], v148 offset:2560\n\t"
    "ds_read_b64_tr_b16 v[132:133], v148 offset:10240\n\t"
    "ds_read_b64_tr_b16 v[134:135], v148 offset:10752\n\t"
    "ds_read_b64_tr_b16 v[140:141], v148 offset:18432\n\t"
    "ds_read_b64_tr_b16 v[142:143], v148 offset:18944\n\t"
    "s_waitcnt lgkmcnt(7)\n\t"
    "v_mfma_f32_32x32x16_bf16 v[64:79], v[120:123], v[112:115], v[64:79]\n\t"
    "v_mfma_f32_32x32x16_bf16 v[80:95], v[128:131], v[112:115], v[80:95]\n\t"
    "v_mfma_f32_32x32x16_bf16 v[96:111], v[112:115], v[136:139], v[96:111]\n\t"
    "ds_read_b128 v[112:115], v146\n\t"
    "ds_read_b64_tr_b16 v[120:121], v148 offset:4096\n\t"
    "ds_read_b64_tr_b16 v[122:123], v148 offset:4608\n\t"
    "ds_read_b64_tr_b16 v[128:129], v148 offset:12288\n\t"
    "ds_read_b64_tr_b16 v[130:131], v148 offset:12800\n\t"
    "ds_read_b64_tr_b16 v[136:137], v148 offset:20480\n\t"
    "ds_read_b64_tr_b16 v[138:139], v148 offset:20992\n\t"
    "s_waitcnt lgkmcnt(7)\n\t"
    "v_mfma_f32_32x32x16_bf16 v[64:79], v[124:127], v[116:119], v[64:79]\n\t"
    "v_mfma_f32_32x32x16_bf16 v[80:95], v[132:135], v[116:119], v[80:95]\n\t"
    "v_mfma_f32_32x32x16_bf16 v[96:111], v[116:119], v[140:143], v[96:111]\n\t"
    "ds_read_b128 v[116:119], v147\n\t"
    "ds_read_b64_tr_b16 v[124:125], v148 offset:6144\n\t"
    "ds_read_b64_tr_b16 v[126:127], v148 offset:6656\n\t"
    "ds_read_b64_tr_b16 v[132:133], v148 offset:14336\n\t"
    "ds_read_b64_tr_b16 v[134:135], v148 offset:14848\n\t"
    "ds_read_b64_tr_b16 v[140:141], v148 offset:22528\n\t"
    "ds_read_b64_tr_b16 v[142:143], v148 offset:23040\n\t"
    "s_waitcnt lgkmcnt(7)\n\t"
    "v_mfma_f32_32x32x16_bf16 v[64:79], v[120:123], v[112:115], v[64:79]\n\t"
    "v_mfma_f32_32x32x16_bf16 v[80:95], v[128:131], v[112:115], v[80:95]\n\t"
    "v_mfma_f32_32x32x16_bf16 v[96:111], v[112:115], v[136:139], v[96:111]\n\t"
    "s_waitcnt lgkmcnt(0)\n\t"
    "v_mfma_f32_32x32x16_bf16 v[64:79], v[124:127], v[116:119], v[64:79]\n\t"
    "v_mfma_f32_32x32x16_bf16 v[80:95], v[132:135], v[116:119], v[80:95]\n\t"
    "v_mfma_f32_32x32x16_bf16 v[96:111], v[116:119], v[140:143], v[96:111]\n\t"
    "v_add_u32 v144, 32768, v144\n\t"
    "v_add_u32 v145, 32768, v145\n\t"
    "v_add_u32 v146, 32768, v146\n\t"
    "v_add_u32 v147, 32768, v147\n\t"
    "v_add_u32 v148, 32768, v148\n\t"
    "s_barrier\n\t"
    "ds_read_b128 v[112:115], v144\n\t"
    "ds_read_b64_tr_b16 v[120:121], v148 offset:0\n\t"
    "ds_read_b64_tr_b16 v[122:123], v148 offset:512\n\t"
    "ds_read_b64_tr_b16 v[128:129], v148 offset:8192\n\t"
    "ds_read_b64_tr_b16 v[130:131], v148 offset:8704\n\t"
    "ds_read_b64_tr_b16 v[136:137], v148 offset:16384\n\t"
    "ds_read_b64_tr_b16 v[138:139], v148 offset:16896\n\t"
    "ds_read_b128 v[116:119], v145\n\t"
    "ds_read_b64_tr_b16 v[124:125], v148 offset:2048\n\t"
    "ds_read_b64_tr_b16 v[126:127], v148 offset:2560\n\t"
    "ds_read_b64_tr_b16 v[132:133], v148 offset:10240\n\t"
    "ds_read_b64_tr_b16 v[134:135], v148 offset:10752\n\t"
    "ds_read_b64_tr_b16 v[140:141], v148 offset:18432\n\t"
    "ds_read_b64_tr_b16 v[142:143], v148 offset:18944\n\t"
    "s_waitcnt lgkmcnt(7)\n\t"
    "v_mfma_f32_32x32x16_bf16 v[64:79], v[120:123], v[112:115], v[64:79]\n\t"
    "v_mfma_f32_32x32x16_bf16 v[80:95], v[128:131], v[112:115], v[80:95]\n\t"
    "v_mfma_f32_32x32x16_bf16 v[96:111], v[112:115], v[136:139], v[96:111]\n\t"
    "ds_read_b128 v[112:115], v146\n\t"
    "ds_read_b64_tr_b16 v[120:121], v148 offset:4096\n\t"
    "ds_read_b64_tr_b16 v[122:123], v148 offset:4608\n\t"
    "ds_read_b64_tr_b16 v[128:129], v148 offset:12288\n\t"
    "ds_read_b64_tr_b16 v[130:131], v148 offset:12800\n\t"
    "ds_read_b64_tr_b16 v[136:137], v148 offset:20480\n\t"
    "ds_read_b64_tr_b16 v[138:139], v148 offset:20992\n\t"
    "s_waitcnt lgkmcnt(7)\n\t"
    "v_mfma_f32_32x32x16_bf16 v[64:79], v[124:127], v[116:119], v[64:79]\n\t"
    "v_mfma_f32_32x32x16_bf16 v[80:95], v[132:135], v[116:119], v[80:95]\n\t"
    "v_mfma_f32_32x32x16_bf16 v[96:111], v[116:119], v[140:143], v[96:111]\n\t"
    "ds_read_b128 v[116:119], v147\n\t"
    "ds_read_b64_tr_b16 v[124:125], v148 offset:6144\n\t"
    "ds_read_b64_tr_b16 v[126:127], v148 offset:6656\n\t"
    "ds_read_b64_tr_b16 v[132:133], v148 offset:14336\n\t"
    "ds_read_b64_tr_b16 v[134:135], v148 offset:14848\n\t"
    "ds_read_b64_tr_b16 v[140:141], v148 offset:22528\n\t"
    "ds_read_b64_tr_b16 v[142:143], v148 offset:23040\n\t"
    "s_waitcnt lgkmcnt(7)\n\t"
    "v_mfma_f32_32x32x16_bf16 v[64:79], v[120:123], v[112:115], v[64:79]\n\t"
    "v_mfma_f32_32x32x16_bf16 v[80:95], v[128:131], v[112:115], v[80:95]\n\t"
    "v_mfma_f32_32x32x16_bf16 v[96:111], v[112:115], v[136:139], v[96:111]\n\t"
    "s_waitcnt lgkmcnt(0)\n\t"
    "v_mfma_f32_32x32x16_bf16 v[64:79], v[124:127], v[116:119], v[64:79]\n\t"
    "v_mfma_f32_32x32x16_bf16 v[80:95], v[132:135], v[116:119], v[80:95]\n\t"
    "v_mfma_f32_32x32x16_bf16 v[96:111], v[116:119], v[140:143], v[96:111]\n\t"
    "v_add_u32 v144, 32768, v144\n\t"
    "v_add_u32 v145, 32768, v145\n\t"
    "v_add_u32 v146, 32768, v146\n\t"
    "v_add_u32 v147, 32768, v147\n\t"
    "v_add_u32 v148, 32768, v148\n\t"
    "s_barrier\n\t"
    "ds_read_b128 v[112:115], v144\n\t"
    "ds_read_b64_tr_b16 v[120:121], v148 offset:0\n\t"
    "ds_read_b64_tr_b16 v[122:123], v148 offset:512\n\t"
    "ds_read_b64_tr_b16 v[128:129], v148 offset:8192\n\t"
    "ds_read_b64_tr_b16 v[130:131], v148 offset:8704\n\t"
    "ds_read_b64_tr_b16 v[136:137], v148 offset:16384\n\t"
    "ds_read_b64_tr_b16 v[138:139], v148 offset:16896\n\t"
    "ds_read_b128 v[116:119], v145\n\t"
    "ds_read_b64_tr_b16 v[124:125], v148 offset:2048\n\t"
    "ds_read_b64_tr_b16 v[126:127], v148 offset:2560\n\t"
    "ds_read_b64_tr_b16 v[132:133], v148 offset:10240\n\t"
    "ds_read_b64_tr_b16 v[134:135], v148 offset:10752\n\t"
    "ds_read_b64_tr_b16 v[140:141], v148 offset:18432\n\t"
    "ds_read_b64_tr_b16 v[142:143], v148 offset:18944\n\t"
    "s_waitcnt lgkmcnt(7)\n\t"
    "v_mfma_f32_32x32x16_bf16 v[64:79], v[120:123], v[112:115], v[64:79]\n\t"
    "v_mfma_f32_32x32x16_bf16 v[80:95], v[128:131], v[112:115], v[80:95]\n\t"
    "v_mfma_f32_32x32x16_bf16 v[96:111], v[112:115], v[136:139], v[96:111]\n\t"
    "ds_read_b128 v[112:115], v146\n\t"
    "ds_read_b64_tr_b16 v[120:121], v148 offset:4096\n\t"
    "ds_read_b64_tr_b16 v[122:123], v148 offset:4608\n\t"
    "ds_read_b64_tr_b16 v[128:129], v148 offset:12288\n\t"
    "ds_read_b64_tr_b16 v[130:131], v148 offset:12800\n\t"
    "ds_read_b64_tr_b16 v[136:137], v148 offset:20480\n\t"
    "ds_read_b64_tr_b16 v[138:139], v148 offset:20992\n\t"
    "s_waitcnt lgkmcnt(7)\n\t"
    "v_mfma_f32_32x32x16_bf16 v[64:79], v[124:127], v[116:119], v[64:79]\n\t"
    "v_mfma_f32_32x32x16_bf16 v[80:95], v[132:135], v[116:119], v[80:95]\n\t"
    "v_mfma_f32_32x32x16_bf16 v[96:111], v[116:119], v[140:143], v[96:111]\n\t"
    "ds_read_b128 v[116:119], v147\n\t"
    "ds_read_b64_tr_b16 v[124:125], v148 offset:6144\n\t"
    "ds_read_b64_tr_b16 v[126:127], v148 offset:6656\n\t"
    "ds_read_b64_tr_b16 v[132:133], v148 offset:14336\n\t"
    "ds_read_b64_tr_b16 v[134:135], v148 offset:14848\n\t"
    "ds_read_b64_tr_b16 v[140:141], v148 offset:22528\n\t"
    "ds_read_b64_tr_b16 v[142:143], v148 offset:23040\n\t"
    "s_waitcnt lgkmcnt(7)\n\t"
    "v_mfma_f32_32x32x16_bf16 v[64:79], v[120:123], v[112:115], v[64:79]\n\t"
    "v_mfma_f32_32x32x16_bf16 v[80:95], v[128:131], v[112:115], v[80:95]\n\t"
    "v_mfma_f32_32x32x16_bf16 v[96:111], v[112:115], v[136:139], v[96:111]\n\t"
    "s_waitcnt lgkmcnt(0)\n\t"
    "v_mfma_f32_32x32x16_bf16 v[64:79], v[124:127], v[116:119], v[64:79]\n\t"
    "v_mfma_f32_32x32x16_bf16 v[80:95], v[132:135], v[116:119], v[80:95]\n\t"
    "v_mfma_f32_32x32x16_bf16 v[96:111], v[116:119], v[140:143], v[96:111]\n\t"
    "v_add_u32 v144, -98304, v144\n\t"
    "v_add_u32 v145, -98304, v145\n\t"
    "v_add_u32 v146, -98304, v146\n\t"
    "v_add_u32 v147, -98304, v147\n\t"
    "v_add_u32 v148, -98304, v148\n\t"
    "s_barrier\n\t"
    "ds_read_b128 v[112:115], v144\n\t"
    "ds_read_b64_tr_b16 v[120:121], v148 offset:0\n\t"
    "ds_read_b64_tr_b16 v[122:123], v148 offset:512\n\t"
    "ds_read_b64_tr_b16 v[128:129], v148 offset:8192\n\t"
    "ds_read_b64_tr_b16 v[130:131], v148 offset:8704\n\t"
    "ds_read_b64_tr_b16 v[136:137], v148 offset:16384\n\t"
    "ds_read_b64_tr_b16 v[138:139], v148 offset:16896\n\t"
    "ds_read_b128 v[116:119], v145\n\t"
    "ds_read_b64_tr_b16 v[124:125], v148 offset:2048\n\t"
    "ds_read_b64_tr_b16 v[126:127], v148 offset:2560\n\t"
    "ds_read_b64_tr_b16 v[132:133], v148 offset:10240\n\t"
    "ds_read_b64_tr_b16 v[134:135], v148 offset:10752\n\t"
    "ds_read_b64_tr_b16 v[140:141], v148 offset:18432\n\t"
    "ds_read_b64_tr_b16 v[142:143], v148 offset:18944\n\t"
    "s_waitcnt lgkmcnt(7)\n\t"
    "v_mfma_f32_32x32x16_bf16 v[64:79], v[120:123], v[112:115], v[64:79]\n\t"
    "v_mfma_f32_32x32x16_bf16 v[80:95], v[128:131], v[112:115], v[80:95]\n\t"
    "v_mfma_f32_32x32x16_bf16 v[96:111], v[112:115], v[136:139], v[96:111]\n\t"
    "ds_read_b128 v[112:115], v146\n\t"
    "ds_read_b64_tr_b16 v[120:121], v148 offset:4096\n\t"
    "ds_read_b64_tr_b16 v[122:123], v148 offset:4608\n\t"
    "ds_read_b64_tr_b16 v[128:129], v148 offset:12288\n\t"
    "ds_read_b64_tr_b16 v[130:131], v148 offset:12800\n\t"
    "ds_read_b64_tr_b16 v[136:137], v148 offset:20480\n\t"
    "ds_read_b64_tr_b16 v[138:139], v148 offset:20992\n\t"
    "s_waitcnt lgkmcnt(7)\n\t"
    "v_mfma_f32_32x32x16_bf16 v[64:79], v[124:127], v[116:119], v[64:79]\n\t"
    "v_mfma_f32_32x32x16_bf16 v[80:95], v[132:135], v[116:119], v[80:95]\n\t"
    "v_mfma_f32_32x32x16_bf16 v[96:111], v[116:119], v[140:143], v[96:111]\n\t"
    "ds_read_b128 v[116:119], v147\n\t"
    "ds_read_b64_tr_b16 v[124:125], v148 offset:6144\n\t"
    "ds_read_b64_tr_b16 v[126:127], v148 offset:6656\n\t"
    "ds_read_b64_tr_b16 v[132:133], v148 offset:14336\n\t"
    "ds_read_b64_tr_b16 v[134:135], v148 offset:14848\n\t"
    "ds_read_b64_tr_b16 v[140:141], v148 offset:22528\n\t"
    "ds_read_b64_tr_b16 v[142:143], v148 offset:23040\n\t"
    "s_waitcnt lgkmcnt(7)\n\t"
    "v_mfma_f32_32x32x16_bf16 v[64:79], v[120:123], v[112:115], v[64:79]\n\t"
    "v_mfma_f32_32x32x16_bf16 v[80:95], v[128:131], v[112:115], v[80:95]\n\t"
    "v_mfma_f32_32x32x16_bf16 v[96:111], v[112:115], v[136:139], v[96:111]\n\t"
    "s_waitcnt lgkmcnt(0)\n\t"
    "v_mfma_f32_32x32x16_bf16 v[64:79], v[124:127], v[116:119], v[64:79]\n\t"
    "v_mfma_f32_32x32x16_bf16 v[80:95], v[132:135], v[116:119], v[80:95]\n\t"
    "v_mfma_f32_32x32x16_bf16 v[96:111], v[116:119], v[140:143], v[96:111]\n\t"
    "v_add_u32 v144, 32768, v144\n\t"
    "v_add_u32 v145, 32768, v145\n\t"
    "v_add_u32 v146, 32768, v146\n\t"
    "v_add_u32 v147, 32768, v147\n\t"
    "v_add_u32 v148, 32768, v148\n\t"
    "s_barrier\n\t"
    "ds_read_b128 v[112:115], v144\n\t"
    "ds_read_b64_tr_b16 v[120:121], v148 offset:0\n\t"
    "ds_read_b64_tr_b16 v[122:123], v148 offset:512\n\t"
    "ds_read_b64_tr_b16 v[128:129], v148 offset:8192\n\t"
    "ds_read_b64_tr_b16 v[130:131], v148 offset:8704\n\t"
    "ds_read_b64_tr_b16 v[136:137], v148 offset:16384\n\t"
    "ds_read_b64_tr_b16 v[138:139], v148 offset:16896\n\t"
    "ds_read_b128 v[116:119], v145\n\t"
    "ds_read_b64_tr_b16 v[124:125], v148 offset:2048\n\t"
    "ds_read_b64_tr_b16 v[126:127], v148 offset:2560\n\t"
    "ds_read_b64_tr_b16 v[132:133], v148 offset:10240\n\t"
    "ds_read_b64_tr_b16 v[134:135], v148 offset:10752\n\t"
    "ds_read_b64_tr_b16 v[140:141], v148 offset:18432\n\t"
    "ds_read_b64_tr_b16 v[142:143], v148 offset:18944\n\t"
    "s_waitcnt lgkmcnt(7)\n\t"
    "v_mfma_f32_32x32x16_bf16 v[64:79], v[120:123], v[112:115], v[64:79]\n\t"
    "v_mfma_f32_32x32x16_bf16 v[80:95], v[128:131], v[112:115], v[80:95]\n\t"
    "v_mfma_f32_32x32x16_bf16 v[96:111], v[112:115], v[136:139], v[96:111]\n\t"
    "ds_read_b128 v[112:115], v146\n\t"
    "ds_read_b64_tr_b16 v[120:121], v148 offset:4096\n\t"
    "ds_read_b64_tr_b16 v[122:123], v148 offset:4608\n\t"
    "ds_read_b64_tr_b16 v[128:129], v148 offset:12288\n\t"
    "ds_read_b64_tr_b16 v[130:131], v148 offset:12800\n\t"
    "ds_read_b64_tr_b16 v[136:137], v148 offset:20480\n\t"
    "ds_read_b64_tr_b16 v[138:139], v148 offset:20992\n\t"
    "s_waitcnt lgkmcnt(7)\n\t"
    "v_mfma_f32_32x32x16_bf16 v[64:79], v[124:127], v[116:119], v[64:79]\n\t"
    "v_mfma_f32_32x32x16_bf16 v[80:95], v[132:135], v[116:119], v[80:95]\n\t"
    "v_mfma_f32_32x32x16_bf16 v[96:111], v[116:119], v[140:143], v[96:111]\n\t"
    "ds_read_b128 v[116:119], v147\n\t"
    "ds_read_b64_tr_b16 v[124:125], v148 offset:6144\n\t"
    "ds_read_b64_tr_b16 v[126:127], v148 offset:6656\n\t"
    "ds_read_b64_tr_b16 v[132:133], v148 offset:14336\n\t"
    "ds_read_b64_tr_b16 v[134:135], v148 offset:14848\n\t"
    "ds_read_b64_tr_b16 v[140:141], v148 offset:22528\n\t"
    "ds_read_b64_tr_b16 v[142:143], v148 offset:23040\n\t"
    "s_waitcnt lgkmcnt(7)\n\t"
    "v_mfma_f32_32x32x16_bf16 v[64:79], v[120:123], v[112:115], v[64:79]\n\t"
    "v_mfma_f32_32x32x16_bf16 v[80:95], v[128:131], v[112:115], v[80:95]\n\t"
    "v_mfma_f32_32x32x16_bf16 v[96:111], v[112:115], v[136:139], v[96:111]\n\t"
    "s_waitcnt lgkmcnt(0)\n\t"
    "v_mfma_f32_32x32x16_bf16 v[64:79], v[124:127], v[116:119], v[64:79]\n\t"
    "v_mfma_f32_32x32x16_bf16 v[80:95], v[132:135], v[116:119], v[80:95]\n\t"
    "v_mfma_f32_32x32x16_bf16 v[96:111], v[116:119], v[140:143], v[96:111]\n\t"
    "v_add_u32 v144, 32768, v144\n\t"
    "v_add_u32 v145, 32768, v145\n\t"
    "v_add_u32 v146, 32768, v146\n\t"
    "v_add_u32 v147, 32768, v147\n\t"
    "v_add_u32 v148, 32768, v148\n\t"
    "s_barrier\n\t"
    "ds_read_b128 v[112:115], v144\n\t"
    "ds_read_b64_tr_b16 v[120:121], v148 offset:0\n\t"
    "ds_read_b64_tr_b16 v[122:123], v148 offset:512\n\t"
    "ds_read_b64_tr_b16 v[128:129], v148 offset:8192\n\t"
    "ds_read_b64_tr_b16 v[130:131], v148 offset:8704\n\t"
    "ds_read_b64_tr_b16 v[136:137], v148 offset:16384\n\t"
    "ds_read_b64_tr_b16 v[138:139], v148 offset:16896\n\t"
    "ds_read_b128 v[116:119], v145\n\t"
    "ds_read_b64_tr_b16 v[124:125], v148 offset:2048\n\t"
    "ds_read_b64_tr_b16 v[126:127], v148 offset:2560\n\t"
    "ds_read_b64_tr_b16 v[132:133], v148 offset:10240\n\t"
    "ds_read_b64_tr_b16 v[134:135], v148 offset:10752\n\t"
    "ds_read_b64_tr_b16 v[140:141], v148 offset:18432\n\t"
    "ds_read_b64_tr_b16 v[142:143], v148 offset:18944\n\t"
    "s_waitcnt lgkmcnt(7)\n\t"
    "v_mfma_f32_32x32x16_bf16 v[64:79], v[120:123], v[112:115], v[64:79]\n\t"
    "v_mfma_f32_32x32x16_bf16 v[80:95], v[128:131], v[112:115], v[80:95]\n\t"
    "v_mfma_f32_32x32x16_bf16 v[96:111], v[112:115], v[136:139], v[96:111]\n\t"
    "ds_read_b128 v[112:115], v146\n\t"
    "ds_read_b64_tr_b16 v[120:121], v148 offset:4096\n\t"
    "ds_read_b64_tr_b16 v[122:123], v148 offset:4608\n\t"
    "ds_read_b64_tr_b16 v[128:129], v148 offset:12288\n\t"
    "ds_read_b64_tr_b16 v[130:131], v148 offset:12800\n\t"
    "ds_read_b64_tr_b16 v[136:137], v148 offset:20480\n\t"
    "ds_read_b64_tr_b16 v[138:139], v148 offset:20992\n\t"
    "s_waitcnt lgkmcnt(7)\n\t"
    "v_mfma_f32_32x32x16_bf16 v[64:79], v[124:127], v[116:119], v[64:79]\n\t"
    "v_mfma_f32_32x32x16_bf16 v[80:95], v[132:135], v[116:119], v[80:95]\n\t"
    "v_mfma_f32_32x32x16_bf16 v[96:111], v[116:119], v[140:143], v[96:111]\n\t"
    "ds_read_b128 v[116:119], v147\n\t"
    "ds_read_b64_tr_b16 v[124:125], v148 offset:6144\n\t"
    "ds_read_b64_tr_b16 v[126:127], v148 offset:6656\n\t"
    "ds_read_b64_tr_b16 v[132:133], v148 offset:14336\n\t"
    "ds_read_b64_tr_b16 v[134:135], v148 offset:14848\n\t"
    "ds_read_b64_tr_b16 v[140:141], v148 offset:22528\n\t"
    "ds_read_b64_tr_b16 v[142:143], v148 offset:23040\n\t"
    "s_waitcnt lgkmcnt(7)\n\t"
    "v_mfma_f32_32x32x16_bf16 v[64:79], v[120:123], v[112:115], v[64:79]\n\t"
    "v_mfma_f32_32x32x16_bf16 v[80:95], v[128:131], v[112:115], v[80:95]\n\t"
    "v_mfma_f32_32x32x16_bf16 v[96:111], v[112:115], v[136:139], v[96:111]\n\t"
    "s_waitcnt lgkmcnt(0)\n\t"
    "v_mfma_f32_32x32x16_bf16 v[64:79], v[124:127], v[116:119], v[64:79]\n\t"
    "v_mfma_f32_32x32x16_bf16 v[80:95], v[132:135], v[116:119], v[80:95]\n\t"
    "v_mfma_f32_32x32x16_bf16 v[96:111], v[116:119], v[140:143], v[96:111]\n\t"
    "s_nop 7\n\t"
    "s_nop 7\n\t"
                    : "=&{v[64:79]}"(aq), "=&{v[80:95]}"(ak), "=&{v[96:111]}"(av)
                    : [ax0] "v"(ax0), [ax1] "v"(ax1), [ax2] "v"(ax2), [ax3] "v"(ax3), [aw] "v"(aw)
                    : "memory", "v112", "v113", "v114", "v115", "v116", "v117", "v118", "v119", "v120", "v121", "v122", "v123", "v124", "v125", "v126", "v127", "v128", "v129", "v130", "v131", "v132", "v133", "v134", "v135", "v136", "v137", "v138", "v139", "v140", "v141", "v142", "v143", "v144", "v145", "v146", "v147", "v148", "v149");
            }

            __builtin_amdgcn_s_setprio(3);
            {
                constexpr float QS = 0.18898223650461361f * 1.4426950408889634f;
#pragma unroll
                for (int mm = 0; mm < 2; ++mm) {
                    const f32x16& a = mm ? aq : ak;
                    const float sc = mm ? QS : 1.f;
                    const unsigned spare0 = mm ? 0u : 0x3f803f80u;
#pragma unroll
                    for (int p = 0; p < 2; ++p) {
                        const unsigned ax = cvtpk(a[8 * p] * sc, a[8 * p + 1] * sc), ay = cvtpk(a[8 * p + 2] * sc, a[8 * p + 3] * sc);
                        const unsigned bx = cvtpk(a[8 * p + 4] * sc, a[8 * p + 5] * sc), by = cvtpk(a[8 * p + 6] * sc, a[8 * p + 7] * sc);
                        const auto rx = __builtin_amdgcn_permlane32_swap(ax, bx, false, false), ry = __builtin_amdgcn_permlane32_swap(ay, by, false, false);
                        st16_sc1(wsr, mm ? q1[p] : k1[p], (u32x4){rx[0], ry[0], spl[p] ? spare0 : rx[1], spl[p] ? 0u : ry[1]});
                        if (__builtin_amdgcn_ballot_w64(spl[p]) != 0) st16_sc1(wsr, mm ? q2[p] : k2[p], (u32x4){rx[1], ry[1], spare0, 0u});
                    }
                    if (mm == 0) {
                        st16_sc1(wsr, vo, __builtin_bit_cast(u32x4, pack8(av, 0))); st16_sc1(wsr, vo + 1024, __builtin_bit_cast(u32x4, pack8(av, 1)));
                        const unsigned one = (lane & 3) == 0 ? 0x3f803f80u : 0u; st16_sc1(wsr, co, (u32x4){one, one, one, one});
                    }
                }
            }

            BAR_LGKM();
            MAKE_TAG();
            WAIT_VM(0);
            unsigned lastw = 0;
            if (lane == 0) {
                lastw = __hip_atomic_fetch_add((LAS unsigned*)(lds + L_ACNT), 1u, __ATOMIC_RELAXED, __HIP_MEMORY_SCOPE_WORKGROUP);
            }
            if (__builtin_amdgcn_readfirstlane(lastw) == 3) {
                const int hh = (64 * dp) / 28 + lane;
                if (hh <= (64 * dp + 63) / 28) {
                    const int second = ((28 * hh) >> 6) != dp;
                    __hip_atomic_store(flags + 32 * (F_KV + 4 * hh + 2 * second + (tp >> 4)) + (tp & 15), tag, RLX_AGENT);
                }
            }
            __builtin_amdgcn_s_setprio(0);
        } else { BAR_LGKM(); MAKE_TAG(); }

        {
            const bool split = ((28 * head) >> 6) != ((28 * head + 27) >> 6);
            group_wait<0>(flags + 32 * (F_KV + 4 * head + (lane >> 4)) + (lane & 15), split || !hi, tag, tmo, wave == 0, lds + L_ACNT + 12);
        }

        const unsigned kvbase = (unsigned)(WS_KV + (head * 64 + wave) * 4096 + lane * 16);
        const i32x4 srdW = make_srd(ws, WS_BYTES);
        bf16x8 Q[4][2];
        u32x4 kv[3][4];
        u32x8 p1z; u32x4 v3a, v3b;
        unsigned rel = (unsigned)rb * 16384u, cnt;
        {
            const unsigned vq = (unsigned)(WS_QF + ((head * 64 + 4 * rb + wave) * 2) * 1024 + lane * 16);
            const unsigned qw = ldsb + L_QX + wave * 2048 + lane * 16;
            asm volatile(
    "buffer_load_dwordx4 v[32:35], %[vq], %[srd], 0 offen offset:0 sc1\n\t"
    "buffer_load_dwordx4 v[36:39], %[vq], %[srd], 0 offen offset:1024 sc1\n\t"
    "buffer_load_dwordx4 v[176:179], %[voff], %[srd], %[rel] offen offset:0 sc1\n\t"
    "buffer_load_dwordx4 v[180:183], %[voff], %[srd], %[rel] offen offset:1024 sc1\n\t"
    "v_mov_b32 v64, 0\n\t"
    "v_mov_b32 v65, 0\n\t"
    "v_mov_b32 v66, 0\n\t"
    "v_mov_b32 v67, 0\n\t"
    "v_mov_b32 v68, 0\n\t"
    "v_mov_b32 v69, 0\n\t"
    "v_mov_b32 v70, 0\n\t"
    "v_mov_b32 v71, 0\n\t"
    "v_mov_b32 v72, 0\n\t"
    "v_mov_b32 v73, 0\n\t"
    "v_mov_b32 v74, 0\n\t"
    "v_mov_b32 v75, 0\n\t"
    "v_mov_b32 v76, 0\n\t"
    "v_mov_b32 v77, 0\n\t"
    "v_mov_b32 v78, 0\n\t"
    "v_mov_b32 v79, 0\n\t"
    "v_mov_b32 v80, 0\n\t"
    "v_mov_b32 v81, 0\n\t"
    "v_mov_b32 v82, 0\n\t"
    "v_mov_b32 v83, 0\n\t"
    "v_mov_b32 v84, 0\n\t"
    "v_mov_b32 v85, 0\n\t"
    "v_mov_b32 v86, 0\n\t"
    "v_mov_b32 v87, 0\n\t"
    "v_mov_b32 v88, 0\n\t"
    "v_mov_b32 v89, 0\n\t"
    "v_mov_b32 v90, 0\n\t"
    "v_mov_b32 v91, 0\n\t"
    "v_mov_b32 v92, 0\n\t"
    "v_mov_b32 v93, 0\n\t"
    "v_mov_b32 v94, 0\n\t"
    "v_mov_b32 v95, 0\n\t"
    "v_mov_b32 v96, 0\n\t"
    "v_mov_b32 v97, 0\n\t"
    "v_mov_b32 v98, 0\n\t"
    "v_mov_b32 v99, 0\n\t"
    "v_mov_b32 v100, 0\n\t"
    "v_mov_b32 v101, 0\n\t"
    "v_mov_b32 v102, 0\n\t"
    "v_mov_b32 v103, 0\n\t"
    "v_mov_b32 v104, 0\n\t"
    "v_mov_b32 v105, 0\n\t"
    "v_mov_b32 v106, 0\n\t"
    "v_mov_b32 v107, 0\n\t"
    "v_mov_b32 v108, 0\n\t"
    "v_mov_b32 v109, 0\n\t"
    "v_mov_b32 v110, 0\n\t"
    "v_mov_b32 v111, 0\n\t"
    "v_mov_b32 v112, 0\n\t"
    "v_mov_b32 v113, 0\n\t"
    "v_mov_b32 v114, 0\n\t"
    "v_mov_b32 v115, 0\n\t"
    "v_mov_b32 v116, 0\n\t"
    "v_mov_b32 v117, 0\n\t"
    "v_mov_b32 v118, 0\n\t"
    "v_mov_b32 v119, 0\n\t"
    "v_mov_b32 v120, 0\n\t"
    "v_mov_b32 v121, 0\n\t"
    "v_mov_b32 v122, 0\n\t"
    "v_mov_b32 v123, 0\n\t"
    "v_mov_b32 v124, 0\n\t"
    "v_mov_b32 v125, 0\n\t"
    "v_mov_b32 v126, 0\n\t"
    "v_mov_b32 v127, 0\n\t"
    "v_mov_b32 v168, 0\n\t"
    "v_mov_b32 v169, 0\n\t"
    "v_mov_b32 v170, 0\n\t"
    "v_mov_b32 v171, 0\n\t"
    "v_mov_b32 v172, 0\n\t"
    "v_mov_b32 v173, 0\n\t"
    "v_mov_b32 v174, 0\n\t"
    "v_mov_b32 v175, 0\n\t"
    "v_mov_b32 v232, 0\n\t"
    "v_mov_b32 v233, 0\n\t"
    "v_mov_b32 v234, 0\n\t"
    "v_mov_b32 v235, 0\n\t"
    "v_mov_b32 v236, 0\n\t"
    "v_mov_b32 v237, 0\n\t"
    "v_mov_b32 v238, 0\n\t"
    "v_mov_b32 v239, 0\n\t"
    "s_waitcnt vmcnt(0)\n\t"
    "ds_write_b128 %[qw], v[32:35]\n\t"
    "ds_write_b128 %[qw], v[36:39] offset:1024\n\t"
    "buffer_load_dwordx4 v[184:187], %[voff], %[srd], %[rel] offen offset:2048 sc1\n\t"
    "buffer_load_dwordx4 v[188:191], %[voff], %[srd], %[rel] offen offset:3072 sc1\n\t"
    "s_add_u32 %[rel], %[rel], 0x4000\n\t"
    "s_and_b32 %[rel], %[rel], 0x3ffff\n\t"
    "buffer_load_dwordx4 v[192:195], %[voff], %[srd], %[rel] offen offset:0 sc1\n\t"
    "buffer_load_dwordx4 v[196:199], %[voff], %[srd], %[rel] offen offset:1024 sc1\n\t"
    "buffer_load_dwordx4 v[200:203], %[voff], %[srd], %[rel] offen offset:2048 sc1\n\t"
    "buffer_load_dwordx4 v[204:207], %[voff], %[srd], %[rel] offen offset:3072 sc1\n\t"
    "s_add_u32 %[rel], %[rel], 0x4000\n\t"
    "s_and_b32 %[rel], %[rel], 0x3ffff\n\t"
    "buffer_load_dwordx4 v[208:211], %[voff], %[srd], %[rel] offen offset:0 sc1\n\t"
    "buffer_load_dwordx4 v[212:215], %[voff], %[srd], %[rel] offen offset:1024 sc1\n\t"
    "buffer_load_dwordx4 v[216:219], %[voff], %[srd], %[rel] offen offset:2048 sc1\n\t"
    "buffer_load_dwordx4 v[220:223], %[voff], %[srd], %[rel] offen offset:3072 sc1\n\t"
    "s_add_u32 %[rel], %[rel], 0x4000\n\t"
    "s_and_b32 %[rel], %[rel], 0x3ffff\n\t"
                : "=&{v[32:35]}"(Q[0][0]), "=&{v[36:39]}"(Q[0][1]),
                  "=&{v[176:179]}"(kv[0][0]), "=&{v[180:183]}"(kv[0][1]), "=&{v[184:187]}"(kv[0][2]), "=&{v[188:191]}"(kv[0][3]),
                  "=&{v[192:195]}"(kv[1][0]), "=&{v[196:199]}"(kv[1][1]), "=&{v[200:203]}"(kv[1][2]), "=&{v[204:207]}"(kv[1][3]),
                  "=&{v[208:211]}"(kv[2][0]), "=&{v[212:215]}"(kv[2][1]), "=&{v[216:219]}"(kv[2][2]), "=&{v[220:223]}"(kv[2][3]),
                  "=&{v[64:79]}"(O[0]), "=&{v[80:95]}"(O[1]), "=&{v[96:111]}"(O[2]), "=&{v[112:127]}"(O[3]), "=&{v[168:175]}"(p1z), "=&{v[232:235]}"(v3a), "=&{v[236:239]}"(v3b),
                  [rel] "+s"(rel)
                : [vq] "v"(vq), [voff] "v"(kvbase), [srd] "s"(srdW), [qw] "v"(qw)
                : "memory", "scc");
        }
        sbar<0>(aw_, ar_, aepoch);
#pragma unroll
        for (int i = 1; i < 4; ++i) { Q[i][0] = lds_b128(lds + L_QX + ((wave + i) & 3) * 2048 + lane * 16); Q[i][1] = lds_b128(lds + L_QX + ((wave + i) & 3) * 2048 + 1024 + lane * 16); }
        const f32x16 zero = {};
        const int sp = (head & 1) ? 1 : 7, sphi = (sp >> 1) & 1, spc = (sp >> 2) * 2 + (sp & 1);
        const bool spme = hi == sphi;
        {
            f32x16 s[4];
#pragma unroll
            for (int qt = 0; qt < 4; ++qt) s[qt] = MFMA(__builtin_bit_cast(bf16x8, kv[0][0]), Q[qt][0], zero);
#pragma unroll
            for (int qt = 0; qt < 4; ++qt) s[qt] = MFMA(__builtin_bit_cast(bf16x8, kv[0][1]), Q[qt][1], s[qt]);
            float m[4];
#pragma unroll
            for (int qt = 0; qt < 4; ++qt) {
                float m0 = __builtin_fmaxf(__builtin_fmaxf(s[qt][0], s[qt][1]), s[qt][2]), m1 = __builtin_fmaxf(__builtin_fmaxf(s[qt][3], s[qt][4]), s[qt][5]);
#pragma unroll
                for (int r = 6; r < 14; r += 4) { m0 = __builtin_fmaxf(__builtin_fmaxf(m0, s[qt][r]), s[qt][r + 1]); m1 = __builtin_fmaxf(__builtin_fmaxf(m1, s[qt][r + 2]), s[qt][r + 3]); }
                m[qt] = __builtin_fmaxf(__builtin_fmaxf(m0, s[qt][14]), __builtin_fmaxf(m1, s[qt][15]));
            }
#pragma unroll
            for (int qt = 0; qt < 4; ++qt) {
                auto rr = __builtin_amdgcn_permlane32_swap(F2U(m[qt]), F2U(m[qt]), false, false);
                const float mm = __builtin_fmaxf(U2F(rr[0]), U2F(rr[1]));
                const float mh = rbf(mm), ml = rbf(mm - mh);
                meff[qt] = mh + ml;
                const unsigned pk = cvtpk(-mh, -ml);
                u32x4 q0 = __builtin_bit_cast(u32x4, Q[qt][0]), q1 = __builtin_bit_cast(u32x4, Q[qt][1]);
                q0[0] = (spme && spc == 0) ? pk : q0[0]; q0[2] = (spme && spc == 1) ? pk : q0[2]; q1[0] = (spme && spc == 2) ? pk : q1[0]; q1[2] = (spme && spc == 3) ? pk : q1[2];
                Q[qt][0] = __builtin_bit_cast(bf16x8, q0); Q[qt][1] = __builtin_bit_cast(bf16x8, q1);
            }
        }

        asm volatile(
    "v_mfma_f32_32x32x16_bf16 v[128:143], v[176:179], v[32:35], 0\n\t"
    "v_mfma_f32_32x32x16_bf16 v[128:143], v[180:183], v[36:39], v[128:143]\n\t"
    "s_mov_b32 %[cnt], 4\n\t"
    "s_nop 7\n\t"
    "s_nop 7\n\t"
    "1:\n\t"
    "v_mfma_f32_32x32x16_bf16 v[144:159], v[176:179], v[40:43], 0\n\t"
    "v_exp_f32 v128, v128\n\t"
    "v_exp_f32 v129, v129\n\t"
    "v_exp_f32 v130, v130\n\t"
    "v_cvt_pk_bf16_f32 v160, v128, v129\n\t"
    "v_exp_f32 v131, v131\n\t"
    "v_exp_f32 v132, v132\n\t"
    "v_cvt_pk_bf16_f32 v161, v130, v131\n\t"
    "v_mfma_f32_32x32x16_bf16 v[144:159], v[180:183], v[44:47], v[144:159]\n\t"
    "v_exp_f32 v133, v133\n\t"
    "v_exp_f32 v134, v134\n\t"
    "v_cvt_pk_bf16_f32 v162, v132, v133\n\t"
    "v_exp_f32 v135, v135\n\t"
    "v_exp_f32 v136, v136\n\t"
    "v_cvt_pk_bf16_f32 v163, v134, v135\n\t"
    "v_mfma_f32_32x32x16_bf16 v[112:127], v[232:235], v[168:171], v[112:127]\n\t"
    "v_exp_f32 v137, v137\n\t"
    "v_exp_f32 v138, v138\n\t"
    "v_cvt_pk_bf16_f32 v164, v136, v137\n\t"
    "v_exp_f32 v139, v139\n\t"
    "v_exp_f32 v140, v140\n\t"
    "v_cvt_pk_bf16_f32 v165, v138, v139\n\t"
    "v_mfma_f32_32x32x16_bf16 v[112:127], v[236:239], v[172:175], v[112:127]\n\t"
    "v_exp_f32 v141, v141\n\t"
    "v_exp_f32 v142, v142\n\t"
    "v_exp_f32 v143, v143\n\t"
    "buffer_load_dwordx4 v[224:227], %[voff], %[srd], %[rel] offen offset:0 sc1\n\t"
    "buffer_load_dwordx4 v[228:231], %[voff], %[srd], %[rel] offen offset:1024 sc1\n\t"
    "buffer_load_dwordx4 v[232:235], %[voff], %[srd], %[rel] offen offset:2048 sc1\n\t"
    "buffer_load_dwordx4 v[236:239], %[voff], %[srd], %[rel] offen offset:3072 sc1\n\t"
    "s_add_u32 %[rel], %[rel], 0x4000\n\t"
    "s_and_b32 %[rel], %[rel], 0x3ffff\n\t"
    "v_cvt_pk_bf16_f32 v166, v140, v141\n\t"
    "v_cvt_pk_bf16_f32 v167, v142, v143\n\t"
    "v_mfma_f32_32x32x16_bf16 v[128:143], v[176:179], v[48:51], 0\n\t"
    "v_exp_f32 v144, v144\n\t"
    "v_exp_f32 v145, v145\n\t"
    "v_exp_f32 v146, v146\n\t"
    "v_cvt_pk_bf16_f32 v168, v144, v145\n\t"
    "v_exp_f32 v147, v147\n\t"
    "v_exp_f32 v148, v148\n\t"
    "v_cvt_pk_bf16_f32 v169, v146, v147\n\t"
    "v_mfma_f32_32x32x16_bf16 v[128:143], v[180:183], v[52:55], v[128:143]\n\t"
    "v_exp_f32 v149, v149\n\t"
    "v_exp_f32 v150, v150\n\t"
    "v_cvt_pk_bf16_f32 v170, v148, v149\n\t"
    "v_exp_f32 v151, v151\n\t"
    "v_exp_f32 v152, v152\n\t"
    "v_cvt_pk_bf16_f32 v171, v150, v151\n\t"
    "s_waitcnt vmcnt(12)\n\t"
    "v_mfma_f32_32x32x16_bf16 v[64:79], v[184:187], v[160:163], v[64:79]\n\t"
    "v_exp_f32 v153, v153\n\t"
    "v_exp_f32 v154, v154\n\t"
    "v_cvt_pk_bf16_f32 v172, v152, v153\n\t"
    "v_exp_f32 v155, v155\n\t"
    "v_exp_f32 v156, v156\n\t"
    "v_cvt_pk_bf16_f32 v173, v154, v155\n\t"
    "v_mfma_f32_32x32x16_bf16 v[64:79], v[188:191], v[164:167], v[64:79]\n\t"
    "v_exp_f32 v157, v157\n\t"
    "v_exp_f32 v158, v158\n\t"
    "v_exp_f32 v159, v159\n\t"
    "v_cvt_pk_bf16_f32 v174, v156, v157\n\t"
    "v_cvt_pk_bf16_f32 v175, v158, v159\n\t"
    "v_mfma_f32_32x32x16_bf16 v[144:159], v[176:179], v[56:59], 0\n\t"
    "v_exp_f32 v128, v128\n\t"
    "v_exp_f32 v129, v129\n\t"
    "v_exp_f32 v130, v130\n\t"
    "v_cvt_pk_bf16_f32 v160, v128, v129\n\t"
    "v_exp_f32 v131, v131\n\t"
    "v_exp_f32 v132, v132\n\t"
    "v_cvt_pk_bf16_f32 v161, v130, v131\n\t"
    "v_mfma_f32_32x32x16_bf16 v[144:159], v[180:183], v[60:63], v[144:159]\n\t"
    "v_exp_f32 v133, v133\n\t"
    "v_exp_f32 v134, v134\n\t"
    "v_cvt_pk_bf16_f32 v162, v132, v133\n\t"
    "v_exp_f32 v135, v135\n\t"
    "v_exp_f32 v136, v136\n\t"
    "v_cvt_pk_bf16_f32 v163, v134, v135\n\t"
    "v_mfma_f32_32x32x16_bf16 v[80:95], v[184:187], v[168:171], v[80:95]\n\t"
    "v_exp_f32 v137, v137\n\t"
    "v_exp_f32 v138, v138\n\t"
    "v_cvt_pk_bf16_f32 v164, v136, v137\n\t"
    "v_exp_f32 v139, v139\n\t"
    "v_exp_f32 v140, v140\n\t"
    "v_cvt_pk_bf16_f32 v165, v138, v139\n\t"
    "v_mfma_f32_32x32x16_bf16 v[80:95], v[188:191], v[172:175], v[80:95]\n\t"
    "v_exp_f32 v141, v141\n\t"
    "v_exp_f32 v142, v142\n\t"
    "v_exp_f32 v143, v143\n\t"
    "v_cvt_pk_bf16_f32 v166, v140, v141\n\t"
    "v_cvt_pk_bf16_f32 v167, v142, v143\n\t"
    "s_waitcnt vmcnt(10)\n\t"
    "v_mfma_f32_32x32x16_bf16 v[128:143], v[192:195], v[32:35], 0\n\t"
    "v_exp_f32 v144, v144\n\t"
    "v_exp_f32 v145, v145\n\t"
    "v_exp_f32 v146, v146\n\t"
    "v_cvt_pk_bf16_f32 v168, v144, v145\n\t"
    "v_exp_f32 v147, v147\n\t"
    "v_exp_f32 v148, v148\n\t"
    "v_cvt_pk_bf16_f32 v169, v146, v147\n\t"
    "v_mfma_f32_32x32x16_bf16 v[128:143], v[196:199], v[36:39], v[128:143]\n\t"
    "v_exp_f32 v149, v149\n\t"
    "v_exp_f32 v150, v150\n\t"
    "v_cvt_pk_bf16_f32 v170, v148, v149\n\t"
    "v_exp_f32 v151, v151\n\t"
    "v_exp_f32 v152, v152\n\t"
    "v_cvt_pk_bf16_f32 v171, v150, v151\n\t"
    "v_mfma_f32_32x32x16_bf16 v[96:111], v[184:187], v[160:163], v[96:111]\n\t"
    "v_exp_f32 v153, v153\n\t"
    "v_exp_f32 v154, v154\n\t"
    "v_cvt_pk_bf16_f32 v172, v152, v153\n\t"
    "v_exp_f32 v155, v155\n\t"
    "v_exp_f32 v156, v156\n\t"
    "v_cvt_pk_bf16_f32 v173, v154, v155\n\t"
    "v_mfma_f32_32x32x16_bf16 v[96:111], v[188:191], v[164:167], v[96:111]\n\t"
    "v_exp_f32 v157, v157\n\t"
    "v_exp_f32 v158, v158\n\t"
    "v_exp_f32 v159, v159\n\t"
    "v_cvt_pk_bf16_f32 v174, v156, v157\n\t"
    "v_cvt_pk_bf16_f32 v175, v158, v159\n\t"
    "v_mfma_f32_32x32x16_bf16 v[144:159], v[192:195], v[40:43], 0\n\t"
    "v_exp_f32 v128, v128\n\t"
    "v_exp_f32 v129, v129\n\t"
    "v_exp_f32 v130, v130\n\t"
    "v_cvt_pk_bf16_f32 v160, v128, v129\n\t"
    "v_exp_f32 v131, v131\n\t"
    "v_exp_f32 v132, v132\n\t"
    "v_cvt_pk_bf16_f32 v161, v130, v131\n\t"
    "v_mfma_f32_32x32x16_bf16 v[144:159], v[196:199], v[44:47], v[144:159]\n\t"
    "v_exp_f32 v133, v133\n\t"
    "v_exp_f32 v134, v134\n\t"
    "v_cvt_pk_bf16_f32 v162, v132, v133\n\t"
    "v_exp_f32 v135, v135\n\t"
    "v_exp_f32 v136, v136\n\t"
    "v_cvt_pk_bf16_f32 v163, v134, v135\n\t"
    "v_mfma_f32_32x32x16_bf16 v[112:127], v[184:187], v[168:171], v[112:127]\n\t"
    "v_exp_f32 v137, v137\n\t"
    "v_exp_f32 v138, v138\n\t"
    "v_cvt_pk_bf16_f32 v164, v136, v137\n\t"
    "v_exp_f32 v139, v139\n\t"
    "v_exp_f32 v140, v140\n\t"
    "v_cvt_pk_bf16_f32 v165, v138, v139\n\t"
    "v_mfma_f32_32x32x16_bf16 v[112:127], v[188:191], v[172:175], v[112:127]\n\t"
    "v_exp_f32 v141, v141\n\t"
    "v_exp_f32 v142, v142\n\t"
    "v_exp_f32 v143, v143\n\t"
    "buffer_load_dwordx4 v[176:179], %[voff], %[srd], %[rel] offen offset:0 sc1\n\t"
    "buffer_load_dwordx4 v[180:183], %[voff], %[srd], %[rel] offen offset:1024 sc1\n\t"
    "buffer_load_dwordx4 v[184:187], %[voff], %[srd], %[rel] offen offset:2048 sc1\n\t"
    "buffer_load_dwordx4 v[188:191], %[voff], %[srd], %[rel] offen offset:3072 sc1\n\t"
    "s_add_u32 %[rel], %[rel], 0x4000\n\t"
    "s_and_b32 %[rel], %[rel], 0x3ffff\n\t"
    "v_cvt_pk_bf16_f32 v166, v140, v141\n\t"
    "v_cvt_pk_bf16_f32 v167, v142, v143\n\t"
    "v_mfma_f32_32x32x16_bf16 v[128:143], v[192:195], v[48:51], 0\n\t"
    "v_exp_f32 v144, v144\n\t"
    "v_exp_f32 v145, v145\n\t"
    "v_exp_f32 v146, v146\n\t"
    "v_cvt_pk_bf16_f32 v168, v144, v145\n\t"
    "v_exp_f32 v147, v147\n\t"
    "v_exp_f32 v148, v148\n\t"
    "v_cvt_pk_bf16_f32 v169, v146, v147\n\t"
    "v_mfma_f32_32x32x16_bf16 v[128:143], v[196:199], v[52:55], v[128:143]\n\t"
    "v_exp_f32 v149, v149\n\t"
    "v_exp_f32 v150, v150\n\t"
    "v_cvt_pk_bf16_f32 v170, v148, v149\n\t"
    "v_exp_f32 v151, v151\n\t"
    "v_exp_f32 v152, v152\n\t"
    "v_cvt_pk_bf16_f32 v171, v150, v151\n\t"
    "s_waitcnt vmcnt(12)\n\t"
    "v_mfma_f32_32x32x16_bf16 v[64:79], v[200:203], v[160:163], v[64:79]\n\t"
    "v_exp_f32 v153, v153\n\t"
    "v_exp_f32 v154, v154\n\t"
    "v_cvt_pk_bf16_f32 v172, v152, v153\n\t"
    "v_exp_f32 v155, v155\n\t"
    "v_exp_f32 v156, v156\n\t"
    "v_cvt_pk_bf16_f32 v173, v154, v155\n\t"
    "v_mfma_f32_32x32x16_bf16 v[64:79], v[204:207], v[164:167], v[64:79]\n\t"
    "v_exp_f32 v157, v157\n\t"
    "v_exp_f32 v158, v158\n\t"
    "v_exp_f32 v159, v159\n\t"
    "v_cvt_pk_bf16_f32 v174, v156, v157\n\t"
    "v_cvt_pk_bf16_f32 v175, v158, v159\n\t"
    "v_mfma_f32_32x32x16_bf16 v[144:159], v[192:195], v[56:59], 0\n\t"
    "v_exp_f32 v128, v128\n\t"
    "v_exp_f32 v129, v129\n\t"
    "v_exp_f32 v130, v130\n\t"
    "v_cvt_pk_bf16_f32 v160, v128, v129\n\t"
    "v_exp_f32 v131, v131\n\t"
    "v_exp_f32 v132, v132\n\t"
    "v_cvt_pk_bf16_f32 v161, v130, v131\n\t"
    "v_mfma_f32_32x32x16_bf16 v[144:159], v[196:199], v[60:63], v[144:159]\n\t"
    "v_exp_f32 v133, v133\n\t"
    "v_exp_f32 v134, v134\n\t"
    "v_cvt_pk_bf16_f32 v162, v132, v133\n\t"
    "v_exp_f32 v135, v135\n\t"
    "v_exp_f32 v136, v136\n\t"
    "v_cvt_pk_bf16_f32 v163, v134, v135\n\t"
    "v_mfma_f32_32x32x16_bf16 v[80:95], v[200:203], v[168:171], v[80:95]\n\t"
    "v_exp_f32 v137, v137\n\t"
    "v_exp_f32 v138, v138\n\t"
    "v_cvt_pk_bf16_f32 v164, v136, v137\n\t"
    "v_exp_f32 v139, v139\n\t"
    "v_exp_f32 v140, v140\n\t"
    "v_cvt_pk_bf16_f32 v165, v138, v139\n\t"
    "v_mfma_f32_32x32x16_bf16 v[80:95], v[204:207], v[172:175], v[80:95]\n\t"
    "v_exp_f32 v141, v141\n\t"
    "v_exp_f32 v142, v142\n\t"
    "v_exp_f32 v143, v143\n\t"
    "v_cvt_pk_bf16_f32 v166, v140, v141\n\t"
    "v_cvt_pk_bf16_f32 v167, v142, v143\n\t"
    "s_waitcnt vmcnt(10)\n\t"
    "v_mfma_f32_32x32x16_bf16 v[128:143], v[208:211], v[32:35], 0\n\t"
    "v_exp_f32 v144, v144\n\t"
    "v_exp_f32 v145, v145\n\t"
    "v_exp_f32 v146, v146\n\t"
    "v_cvt_pk_bf16_f32 v168, v144, v145\n\t"
    "v_exp_f32 v147, v147\n\t"
    "v_exp_f32 v148, v148\n\t"
    "v_cvt_pk_bf16_f32 v169, v146, v147\n\t"
    "v_mfma_f32_32x32x16_bf16 v[128:143], v[212:215], v[36:39], v[128:143]\n\t"
    "v_exp_f32 v149, v149\n\t"
    "v_exp_f32 v150, v150\n\t"
    "v_cvt_pk_bf16_f32 v170, v148, v149\n\t"
    "v_exp_f32 v151, v151\n\t"
    "v_exp_f32 v152, v152\n\t"
    "v_cvt_pk_bf16_f32 v171, v150, v151\n\t"
    "v_mfma_f32_32x32x16_bf16 v[96:111], v[200:203], v[160:163], v[96:111]\n\t"
    "v_exp_f32 v153, v153\n\t"
    "v_exp_f32 v154, v154\n\t"
    "v_cvt_pk_bf16_f32 v172, v152, v153\n\t"
    "v_exp_f32 v155, v155\n\t"
    "v_exp_f32 v156, v156\n\t"
    "v_cvt_pk_bf16_f32 v173, v154, v155\n\t"
    "v_mfma_f32_32x32x16_bf16 v[96:111], v[204:207], v[164:167], v[96:111]\n\t"
    "v_exp_f32 v157, v157\n\t"
    "v_exp_f32 v158, v158\n\t"
    "v_exp_f32 v159, v159\n\t"
    "v_cvt_pk_bf16_f32 v174, v156, v157\n\t"
    "v_cvt_pk_bf16_f32 v175, v158, v159\n\t"
    "v_mfma_f32_32x32x16_bf16 v[144:159], v[208:211], v[40:43], 0\n\t"
    "v_exp_f32 v128, v128\n\t"
    "v_exp_f32 v129, v129\n\t"
    "v_exp_f32 v130, v130\n\t"
    "v_cvt_pk_bf16_f32 v160, v128, v129\n\t"
    "v_exp_f32 v131, v131\n\t"
    "v_exp_f32 v132, v132\n\t"
    "v_cvt_pk_bf16_f32 v161, v130, v131\n\t"
    "v_mfma_f32_32x32x16_bf16 v[144:159], v[212:215], v[44:47], v[144:159]\n\t"
    "v_exp_f32 v133, v133\n\t"
    "v_exp_f32 v134, v134\n\t"
    "v_cvt_pk_bf16_f32 v162, v132, v133\n\t"
    "v_exp_f32 v135, v135\n\t"
    "v_exp_f32 v136, v136\n\t"
    "v_cvt_pk_bf16_f32 v163, v134, v135\n\t"
    "v_mfma_f32_32x32x16_bf16 v[112:127], v[200:203], v[168:171], v[112:127]\n\t"
    "v_exp_f32 v137, v137\n\t"
    "v_exp_f32 v138, v138\n\t"
    "v_cvt_pk_bf16_f32 v164, v136, v137\n\t"
    "v_exp_f32 v139, v139\n\t"
    "v_exp_f32 v140, v140\n\t"
    "v_cvt_pk_bf16_f32 v165, v138, v139\n\t"
    "v_mfma_f32_32x32x16_bf16 v[112:127], v[204:207], v[172:175], v[112:127]\n\t"
    "v_exp_f32 v141, v141\n\t"
    "v_exp_f32 v142, v142\n\t"
    "v_exp_f32 v143, v143\n\t"
    "buffer_load_dwordx4 v[192:195], %[voff], %[srd], %[rel] offen offset:0 sc1\n\t"
    "buffer_load_dwordx4 v[196:199], %[voff], %[srd], %[rel] offen offset:1024 sc1\n\t"
    "buffer_load_dwordx4 v[200:203], %[voff], %[srd], %[rel] offen offset:2048 sc1\n\t"
    "buffer_load_dwordx4 v[204:207], %[voff], %[srd], %[rel] offen offset:3072 sc1\n\t"
    "s_add_u32 %[rel], %[rel], 0x4000\n\t"
    "s_and_b32 %[rel], %[rel], 0x3ffff\n\t"
    "v_cvt_pk_bf16_f32 v166, v140, v141\n\t"
    "v_cvt_pk_bf16_f32 v167, v142, v143\n\t"
    "v_mfma_f32_32x32x16_bf16 v[128:143], v[208:211], v[48:51], 0\n\t"
    "v_exp_f32 v144, v144\n\t"
    "v_exp_f32 v145, v145\n\t"
    "v_exp_f32 v146, v146\n\t"
    "v_cvt_pk_bf16_f32 v168, v144, v145\n\t"
    "v_exp_f32 v147, v147\n\t"
    "v_exp_f32 v148, v148\n\t"
    "v_cvt_pk_bf16_f32 v169, v146, v147\n\t"
    "v_mfma_f32_32x32x16_bf16 v[128:143], v[212:215], v[52:55], v[128:143]\n\t"
    "v_exp_f32 v149, v149\n\t"
    "v_exp_f32 v150, v150\n\t"
    "v_cvt_pk_bf16_f32 v170, v148, v149\n\t"
    "v_exp_f32 v151, v151\n\t"
    "v_exp_f32 v152, v152\n\t"
    "v_cvt_pk_bf16_f32 v171, v150, v151\n\t"
    "s_waitcnt vmcnt(12)\n\t"
    "v_mfma_f32_32x32x16_bf16 v[64:79], v[216:219], v[160:163], v[64:79]\n\t"
    "v_exp_f32 v153, v153\n\t"
    "v_exp_f32 v154, v154\n\t"
    "v_cvt_pk_bf16_f32 v172, v152, v153\n\t"
    "v_exp_f32 v155, v155\n\t"
    "v_exp_f32 v156, v156\n\t"
    "v_cvt_pk_bf16_f32 v173, v154, v155\n\t"
    "v_mfma_f32_32x32x16_bf16 v[64:79], v[220:223], v[164:167], v[64:79]\n\t"
    "v_exp_f32 v157, v157\n\t"
    "v_exp_f32 v158, v158\n\t"
    "v_exp_f32 v159, v159\n\t"
    "v_cvt_pk_bf16_f32 v174, v156, v157\n\t"
    "v_cvt_pk_bf16_f32 v175, v158, v159\n\t"
    "v_mfma_f32_32x32x16_bf16 v[144:159], v[208:211], v[56:59], 0\n\t"
    "v_exp_f32 v128, v128\n\t"
    "v_exp_f32 v129, v129\n\t"
    "v_exp_f32 v130, v130\n\t"
    "v_cvt_pk_bf16_f32 v160, v128, v129\n\t"
    "v_exp_f32 v131, v131\n\t"
    "v_exp_f32 v132, v132\n\t"
    "v_cvt_pk_bf16_f32 v161, v130, v131\n\t"
    "v_mfma_f32_32x32x16_bf16 v[144:159], v[212:215], v[60:63], v[144:159]\n\t"
    "v_exp_f32 v133, v133\n\t"
    "v_exp_f32 v134, v134\n\t"
    "v_cvt_pk_bf16_f32 v162, v132, v133\n\t"
    "v_exp_f32 v135, v135\n\t"
    "v_exp_f32 v136, v136\n\t"
    "v_cvt_pk_bf16_f32 v163, v134, v135\n\t"
    "v_mfma_f32_32x32x16_bf16 v[80:95], v[216:219], v[168:171], v[80:95]\n\t"
    "v_exp_f32 v137, v137\n\t"
    "v_exp_f32 v138, v138\n\t"
    "v_cvt_pk_bf16_f32 v164, v136, v137\n\t"
    "v_exp_f32 v139, v139\n\t"
    "v_exp_f32 v140, v140\n\t"
    "v_cvt_pk_bf16_f32 v165, v138, v139\n\t"
    "v_mfma_f32_32x32x16_bf16 v[80:95], v[220:223], v[172:175], v[80:95]\n\t"
    "v_exp_f32 v141, v141\n\t"
    "v_exp_f32 v142, v142\n\t"
    "v_exp_f32 v143, v143\n\t"
    "v_cvt_pk_bf16_f32 v166, v140, v141\n\t"
    "v_cvt_pk_bf16_f32 v167, v142, v143\n\t"
    "s_waitcnt vmcnt(10)\n\t"
    "v_mfma_f32_32x32x16_bf16 v[128:143], v[224:227], v[32:35], 0\n\t"
    "v_exp_f32 v144, v144\n\t"
    "v_exp_f32 v145, v145\n\t"
    "v_exp_f32 v146, v146\n\t"
    "v_cvt_pk_bf16_f32 v168, v144, v145\n\t"
    "v_exp_f32 v147, v147\n\t"
    "v_exp_f32 v148, v148\n\t"
    "v_cvt_pk_bf16_f32 v169, v146, v147\n\t"
    "v_mfma_f32_32x32x16_bf16 v[128:143], v[228:231], v[36:39], v[128:143]\n\t"
    "v_exp_f32 v149, v149\n\t"
    "v_exp_f32 v150, v150\n\t"
    "v_cvt_pk_bf16_f32 v170, v148, v149\n\t"
    "v_exp_f32 v151, v151\n\t"
    "v_exp_f32 v152, v152\n\t"
    "v_cvt_pk_bf16_f32 v171, v150, v151\n\t"
    "v_mfma_f32_32x32x16_bf16 v[96:111], v[216:219], v[160:163], v[96:111]\n\t"
    "v_exp_f32 v153, v153\n\t"
    "v_exp_f32 v154, v154\n\t"
    "v_cvt_pk_bf16_f32 v172, v152, v153\n\t"
    "v_exp_f32 v155, v155\n\t"
    "v_exp_f32 v156, v156\n\t"
    "v_cvt_pk_bf16_f32 v173, v154, v155\n\t"
    "v_mfma_f32_32x32x16_bf16 v[96:111], v[220:223], v[164:167], v[96:111]\n\t"
    "v_exp_f32 v157, v157\n\t"
    "v_exp_f32 v158, v158\n\t"
    "v_exp_f32 v159, v159\n\t"
    "v_cvt_pk_bf16_f32 v174, v156, v157\n\t"
    "v_cvt_pk_bf16_f32 v175, v158, v159\n\t"
    "v_mfma_f32_32x32x16_bf16 v[144:159], v[224:227], v[40:43], 0\n\t"
    "v_exp_f32 v128, v128\n\t"
    "v_exp_f32 v129, v129\n\t"
    "v_exp_f32 v130, v130\n\t"
    "v_cvt_pk_bf16_f32 v160, v128, v129\n\t"
    "v_exp_f32 v131, v131\n\t"
    "v_exp_f32 v132, v132\n\t"
    "v_cvt_pk_bf16_f32 v161, v130, v131\n\t"
    "v_mfma_f32_32x32x16_bf16 v[144:159], v[228:231], v[44:47], v[144:159]\n\t"
    "v_exp_f32 v133, v133\n\t"
    "v_exp_f32 v134, v134\n\t"
    "v_cvt_pk_bf16_f32 v162, v132, v133\n\t"
    "v_exp_f32 v135, v135\n\t"
    "v_exp_f32 v136, v136\n\t"
    "v_cvt_pk_bf16_f32 v163, v134, v135\n\t"
    "v_mfma_f32_32x32x16_bf16 v[112:127], v[216:219], v[168:171], v[112:127]\n\t"
    "v_exp_f32 v137, v137\n\t"
    "v_exp_f32 v138, v138\n\t"
    "v_cvt_pk_bf16_f32 v164, v136, v137\n\t"
    "v_exp_f32 v139, v139\n\t"
    "v_exp_f32 v140, v140\n\t"
    "v_cvt_pk_bf16_f32 v165, v138, v139\n\t"
    "v_mfma_f32_32x32x16_bf16 v[112:127], v[220:223], v[172:175], v[112:127]\n\t"
    "v_exp_f32 v141, v141\n\t"
    "v_exp_f32 v142, v142\n\t"
    "v_exp_f32 v143, v143\n\t"
    "buffer_load_dwordx4 v[208:211], %[voff], %[srd], %[rel] offen offset:0 sc1\n\t"
    "buffer_load_dwordx4 v[212:215], %[voff], %[srd], %[rel] offen offset:1024 sc1\n\t"
    "buffer_load_dwordx4 v[216:219], %[voff], %[srd], %[rel] offen offset:2048 sc1\n\t"
    "buffer_load_dwordx4 v[220:223], %[voff], %[srd], %[rel] offen offset:3072 sc1\n\t"
    "s_add_u32 %[rel], %[rel], 0x4000\n\t"
    "s_and_b32 %[rel], %[rel], 0x3ffff\n\t"
    "v_cvt_pk_bf16_f32 v166, v140, v141\n\t"
    "v_cvt_pk_bf16_f32 v167, v142, v143\n\t"
    "v_mfma_f32_32x32x16_bf16 v[128:143], v[224:227], v[48:51], 0\n\t"
    "v_exp_f32 v144, v144\n\t"
    "v_exp_f32 v145, v145\n\t"
    "v_exp_f32 v146, v146\n\t"
    "v_cvt_pk_bf16_f32 v168, v144, v145\n\t"
    "v_exp_f32 v147, v147\n\t"
    "v_exp_f32 v148, v148\n\t"
    "v_cvt_pk_bf16_f32 v169, v146, v147\n\t"
    "v_mfma_f32_32x32x16_bf16 v[128:143], v[228:231], v[52:55], v[128:143]\n\t"
    "v_exp_f32 v149, v149\n\t"
    "v_exp_f32 v150, v150\n\t"
    "v_cvt_pk_bf16_f32 v170, v148, v149\n\t"
    "v_exp_f32 v151, v151\n\t"
    "v_exp_f32 v152, v152\n\t"
    "v_cvt_pk_bf16_f32 v171, v150, v151\n\t"
    "s_waitcnt vmcnt(12)\n\t"
    "v_mfma_f32_32x32x16_bf16 v[64:79], v[232:235], v[160:163], v[64:79]\n\t"
    "v_exp_f32 v153, v153\n\t"
    "v_exp_f32 v154, v154\n\t"
    "v_cvt_pk_bf16_f32 v172, v152, v153\n\t"
    "v_exp_f32 v155, v155\n\t"
    "v_exp_f32 v156, v156\n\t"
    "v_cvt_pk_bf16_f32 v173, v154, v155\n\t"
    "v_mfma_f32_32x32x16_bf16 v[64:79], v[236:239], v[164:167], v[64:79]\n\t"
    "v_exp_f32 v157, v157\n\t"
    "v_exp_f32 v158, v158\n\t"
    "v_exp_f32 v159, v159\n\t"
    "v_cvt_pk_bf16_f32 v174, v156, v157\n\t"
    "v_cvt_pk_bf16_f32 v175, v158, v159\n\t"
    "v_mfma_f32_32x32x16_bf16 v[144:159], v[224:227], v[56:59], 0\n\t"
    "v_exp_f32 v128, v128\n\t"
    "v_exp_f32 v129, v129\n\t"
    "v_exp_f32 v130, v130\n\t"
    "v_cvt_pk_bf16_f32 v160, v128, v129\n\t"
    "v_exp_f32 v131, v131\n\t"
    "v_exp_f32 v132, v132\n\t"
    "v_cvt_pk_bf16_f32 v161, v130, v131\n\t"
    "v_mfma_f32_32x32x16_bf16 v[144:159], v[228:231], v[60:63], v[144:159]\n\t"
    "v_exp_f32 v133, v133\n\t"
    "v_exp_f32 v134, v134\n\t"
    "v_cvt_pk_bf16_f32 v162, v132, v133\n\t"
    "v_exp_f32 v135, v135\n\t"
    "v_exp_f32 v136, v136\n\t"
    "v_cvt_pk_bf16_f32 v163, v134, v135\n\t"
    "v_mfma_f32_32x32x16_bf16 v[80:95], v[232:235], v[168:171], v[80:95]\n\t"
    "v_exp_f32 v137, v137\n\t"
    "v_exp_f32 v138, v138\n\t"
    "v_cvt_pk_bf16_f32 v164, v136, v137\n\t"
    "v_exp_f32 v139, v139\n\t"
    "v_exp_f32 v140, v140\n\t"
    "v_cvt_pk_bf16_f32 v165, v138, v139\n\t"
    "v_mfma_f32_32x32x16_bf16 v[80:95], v[236:239], v[172:175], v[80:95]\n\t"
    "v_exp_f32 v141, v141\n\t"
    "v_exp_f32 v142, v142\n\t"
    "v_exp_f32 v143, v143\n\t"
    "v_cvt_pk_bf16_f32 v166, v140, v141\n\t"
    "v_cvt_pk_bf16_f32 v167, v142, v143\n\t"
    "s_waitcnt vmcnt(10)\n\t"
    "v_mfma_f32_32x32x16_bf16 v[128:143], v[176:179], v[32:35], 0\n\t"
    "v_exp_f32 v144, v144\n\t"
    "v_exp_f32 v145, v145\n\t"
    "v_exp_f32 v146, v146\n\t"
    "v_cvt_pk_bf16_f32 v168, v144, v145\n\t"
    "v_exp_f32 v147, v147\n\t"
    "v_exp_f32 v148, v148\n\t"
    "v_cvt_pk_bf16_f32 v169, v146, v147\n\t"
    "v_mfma_f32_32x32x16_bf16 v[128:143], v[180:183], v[36:39], v[128:143]\n\t"
    "v_exp_f32 v149, v149\n\t"
    "v_exp_f32 v150, v150\n\t"
    "v_cvt_pk_bf16_f32 v170, v148, v149\n\t"
    "v_exp_f32 v151, v151\n\t"
    "v_exp_f32 v152, v152\n\t"
    "v_cvt_pk_bf16_f32 v171, v150, v151\n\t"
    "v_mfma_f32_32x32x16_bf16 v[96:111], v[232:235], v[160:163], v[96:111]\n\t"
    "v_exp_f32 v153, v153\n\t"
    "v_exp_f32 v154, v154\n\t"
    "v_cvt_pk_bf16_f32 v172, v152, v153\n\t"
    "v_exp_f32 v155, v155\n\t"
    "v_exp_f32 v156, v156\n\t"
    "v_cvt_pk_bf16_f32 v173, v154, v155\n\t"
    "v_mfma_f32_32x32x16_bf16 v[96:111], v[236:239], v[164:167], v[96:111]\n\t"
    "v_exp_f32 v157, v157\n\t"
    "v_exp_f32 v158, v158\n\t"
    "v_exp_f32 v159, v159\n\t"
    "v_cvt_pk_bf16_f32 v174, v156, v157\n\t"
    "v_cvt_pk_bf16_f32 v175, v158, v159\n\t"
    "s_sub_u32 %[cnt], %[cnt], 1\n\t"
    "s_cmp_lg_u32 %[cnt], 0\n\t"
    "s_cbranch_scc1 1b\n\t"
    "s_nop 1\n\t"
    "v_mfma_f32_32x32x16_bf16 v[112:127], v[232:235], v[168:171], v[112:127]\n\t"
    "v_mfma_f32_32x32x16_bf16 v[112:127], v[236:239], v[172:175], v[112:127]\n\t"
    "s_waitcnt vmcnt(0)\n\t"
    "s_nop 7\n\t"
    "s_nop 7\n\t"
            : "+{v[64:79]}"(O[0]), "+{v[80:95]}"(O[1]), "+{v[96:111]}"(O[2]), "+{v[112:127]}"(O[3]), "+{v[168:175]}"(p1z), "+{v[232:235]}"(v3a), "+{v[236:239]}"(v3b),
              "+{v[176:179]}"(kv[0][0]), "+{v[180:183]}"(kv[0][1]), "+{v[184:187]}"(kv[0][2]), "+{v[188:191]}"(kv[0][3]),
              "+{v[192:195]}"(kv[1][0]), "+{v[196:199]}"(kv[1][1]), "+{v[200:203]}"(kv[1][2]), "+{v[204:207]}"(kv[1][3]),
              "+{v[208:211]}"(kv[2][0]), "+{v[212:215]}"(kv[2][1]), "+{v[216:219]}"(kv[2][2]), "+{v[220:223]}"(kv[2][3]),
              [rel] "+s"(rel), [cnt] "=&s"(cnt)
            : "{v[32:35]}"(Q[0][0]), "{v[36:39]}"(Q[0][1]), "{v[40:43]}"(Q[1][0]), "{v[44:47]}"(Q[1][1]), "{v[48:51]}"(Q[2][0]), "{v[52:55]}"(Q[2][1]), "{v[56:59]}"(Q[3][0]), "{v[60:63]}"(Q[3][1]),
              [voff] "v"(kvbase), [srd] "s"(srdW)
            : "memory", "scc", "v128", "v129", "v130", "v131", "v132", "v133", "v134", "v135", "v136", "v137", "v138", "v139", "v140", "v141", "v142", "v143", "v144", "v145", "v146", "v147", "v148", "v149", "v150", "v151", "v152", "v153", "v154", "v155", "v156", "v157", "v158", "v159", "v160", "v161", "v162", "v163", "v164", "v165", "v166", "v167", "v224", "v225", "v226", "v227", "v228", "v229", "v230", "v231");

        if (lane == 0) __hip_atomic_fetch_add((LAS unsigned*)(lds + L_ACNT + 28), 1u, __ATOMIC_RELAXED, __HIP_MEMORY_SCOPE_WORKGROUP);
        bool bad = false;
#pragma unroll
        for (int qt = 0; qt < 4; ++qt) bad |= hi && !(O[qt][12] < 1.0995116e12f);
        if (__builtin_amdgcn_ballot_w64(bad) != 0) {
#pragma unroll
            for (int qt = 0; qt < 4; ++qt) {
                u32x4 q0 = __builtin_bit_cast(u32x4, Q[qt][0]), q1 = __builtin_bit_cast(u32x4, Q[qt][1]);
                q0[0] = (spme && spc == 0) ? 0u : q0[0]; q0[2] = (spme && spc == 1) ? 0u : q0[2]; q1[0] = (spme && spc == 2) ? 0u : q1[0]; q1[2] = (spme && spc == 3) ? 0u : q1[2];
                Q[qt][0] = __builtin_bit_cast(bf16x8, q0); Q[qt][1] = __builtin_bit_cast(bf16x8, q1);
                O[qt] = zero; meff[qt] = -3.0e38f;
            }
#pragma unroll 1
            for (int j = 0; j < 16; ++j) {
                const unsigned off = kvbase + j * 16384;
                const bf16x8 k0 = __builtin_bit_cast(bf16x8, ld16_sc1(wsr, off)), k1 = __builtin_bit_cast(bf16x8, ld16_sc1(wsr, off + 1024));
                const bf16x8 v0 = __builtin_bit_cast(bf16x8, ld16_sc1(wsr, off + 2048)), v1 = __builtin_bit_cast(bf16x8, ld16_sc1(wsr, off + 3072));
#pragma unroll
                for (int qt = 0; qt < 4; ++qt) {
                    f32x16 s = MFMA(k0, Q[qt][0], zero); s = MFMA(k1, Q[qt][1], s);
                    float m = s[0];
#pragma unroll
                    for (int r = 1; r < 16; ++r) m = __builtin_fmaxf(m, s[r]);
                    { auto rr = __builtin_amdgcn_permlane32_swap(F2U(m), F2U(m), false, false); m = __builtin_fmaxf(U2F(rr[0]), U2F(rr[1])); }
                    const float mn = __builtin_fmaxf(meff[qt], m), al = __builtin_amdgcn_exp2f(meff[qt] - mn);
                    meff[qt] = mn;
#pragma unroll
                    for (int r = 0; r < 16; ++r) { s[r] = __builtin_amdgcn_exp2f(s[r] - mn); O[qt][r] *= al; }
                    O[qt] = MFMA(v0, pack8(s, 0), O[qt]); O[qt] = MFMA(v1, pack8(s, 1), O[qt]);
                }
            }
        }
    } else {
        const int sw = wave - 4;
        const unsigned cntw = ldsb + L_BARS + sw * 128 + r32 * 4, cntr = ldsb + L_BARS + (lane & 3) * 128;
        const int pch = 32 * g + r32 < 448 ? 32 * g + r32 : 447;
        unsigned lamw = LamRe[pch], lamw2 = LamIm[pch], dtw = logdt[pch];
      if (b >= 224) {
          unsigned long long pc; asm volatile("s_getpc_b64 %0" : "=s"(pc));
          const GAS unsigned* cp = (const GAS unsigned*)((pc & ~255ull) - CODE_BEFORE) + (((b >> 3) & 3) * 4 + sw) * (CODE_BYTES / 64) + lane * 16;
          unsigned acc = 0;
#pragma unroll
          for (int i = 0; i < CODE_BYTES / 16 / 4096 + 1; ++i) if (lane * 64 + i * 4096 < CODE_BYTES / 16) acc ^= cp[i * 1024];
          asm volatile("s_barrier" ::: "memory");
        const unsigned vo = (unsigned)((((b >> 3) & 3) * 4 + wave - 4) * 1024 + lane * 16), ld = ldsb + wave * 1024;
        {
            const i32x4 s0 = make_srd(xA + (size_t)(512 * (xj >> 1)) * DM, 512 * DM * 2), s1 = make_srd(Bre + (size_t)(224 * (xj & 1)) * DM, 224 * DM * 2), s2 = make_srd(Bim + (size_t)(224 * (xj & 1)) * DM, 224 * DM * 2);
#pragma unroll 1
            for (int k = 0; k < 14; ++k) { dma16(s0, vo, k * 32768, ld); dma16(s0, vo, k * 32768 + 16384, ld); dma16(s1, vo, k * 16384, ld); dma16(s2, vo, k * 16384, ld); WAIT_VM(6); }
        }
        {
            const i32x4 s0 = make_srd(Cre, DM * DM * 2), s1 = make_srd(Cim, DM * DM * 2), s2 = make_srd(Wo, DM * DM * 2);
#pragma unroll 1
            for (int k = 0; k < 25; ++k) { dma16(s0, vo, k * 16384, ld); dma16(s1, vo, k * 16384, ld); dma16(s2, vo, k * 16384, ld); WAIT_VM(0); }
        }
          asm volatile("" :: "v"(acc));
          MAKE_TAG();
      }
      else {
        const int r0 = 32 * sw + (lane >> 3);
        const unsigned axe = (unsigned)((128 * c + r0) * 896 + (((lane & 7) ^ ((r0 >> 1) & 3)) << 4)), axo = (unsigned)((128 * c + r0 + 8) * 896 + (((lane & 7) ^ (((r0 + 8) >> 1) & 3)) << 4));
        const int brow = 8 * sw + (lane >> 3);
        const unsigned vb = (unsigned)((32 * g + brow) * 896 + (((lane & 7) ^ ((brow >> 1) & 3)) << 4));
        const unsigned la = ldsb + sw * 4096, lb = ldsb + 16384 + sw * 1024;
        unsigned ks = (unsigned)(((idx >> 2) + 2 * (idx & 3)) % 7) * 128u;
        const i32x4 srdA = make_srd(xA, SEQ * DM * 2), srdR = make_srd(Bre, DM * DM * 2), srdI = make_srd(Bim, DM * DM * 2);
        {
            const i32x4 srdX = make_srd(xB, SEQ * DM * 2), srdW = make_srd(Wqkv, DM * 3 * DM * 2);
            const int xr = 16 * sw + (lane >> 3);
            const unsigned vxe = (unsigned)((64 * tp + xr) * 896 + (((lane & 7) ^ ((xr >> 1) & 3)) << 4)), vxo = (unsigned)((64 * tp + xr + 8) * 896 + (((lane & 7) ^ (((xr + 8) >> 1) & 3)) << 4));
            const unsigned vw = (unsigned)(xr * 2688 + dp * 128 + (((lane & 7) ^ (((xr >> 1) & 1) << 2)) << 4));
            const unsigned lx = ldsb + sw * 2048;
            unsigned t0, t1, m0s;
            unsigned kx = (unsigned)(((tp & 7) + 2 * dp) % 7) * 128u;
            asm volatile(
    "s_mov_b32 %[m0s], m0\n\t"
    "s_add_u32 m0, %[lx], 0\n\t"
    "s_mul_i32 %[t0], %[kx], 1344\n\t"
    "buffer_load_dwordx4 %[vxe], %[srdx], %[kx] offen lds\n\t"
    "s_add_u32 m0, %[lx], 1024\n\t"
    "s_nop 0\n\t"
    "buffer_load_dwordx4 %[vxo], %[srdx], %[kx] offen lds\n\t"
    "s_add_u32 m0, %[lx], 8192\n\t"
    "s_add_u32 %[t1], %[t0], 0\n\t"
    "buffer_load_dwordx4 %[vw], %[srdw], %[t1] offen lds\n\t"
    "s_add_u32 m0, %[lx], 9216\n\t"
    "s_add_u32 %[t1], %[t0], 21504\n\t"
    "buffer_load_dwordx4 %[vw], %[srdw], %[t1] offen lds\n\t"
    "s_add_u32 m0, %[lx], 16384\n\t"
    "s_add_u32 %[t1], %[t0], 896\n\t"
    "buffer_load_dwordx4 %[vw], %[srdw], %[t1] offen lds\n\t"
    "s_add_u32 m0, %[lx], 17408\n\t"
    "s_add_u32 %[t1], %[t0], 22400\n\t"
    "buffer_load_dwordx4 %[vw], %[srdw], %[t1] offen lds\n\t"
    "s_add_u32 m0, %[lx], 24576\n\t"
    "s_add_u32 %[t1], %[t0], 1792\n\t"
    "buffer_load_dwordx4 %[vw], %[srdw], %[t1] offen lds\n\t"
    "s_add_u32 m0, %[lx], 25600\n\t"
    "s_add_u32 %[t1], %[t0], 23296\n\t"
    "buffer_load_dwordx4 %[vw], %[srdw], %[t1] offen lds\n\t"
    "s_add_u32 %[kx], %[kx], 128\n\t"
    "s_cmp_eq_u32 %[kx], 896\n\t"
    "s_cselect_b32 %[kx], 0, %[kx]\n\t"
    "s_barrier\n\t"
    "s_add_u32 m0, %[lx], 32768\n\t"
    "s_mul_i32 %[t0], %[kx], 1344\n\t"
    "buffer_load_dwordx4 %[vxe], %[srdx], %[kx] offen lds\n\t"
    "s_add_u32 m0, %[lx], 33792\n\t"
    "s_nop 0\n\t"
    "buffer_load_dwordx4 %[vxo], %[srdx], %[kx] offen lds\n\t"
    "s_add_u32 m0, %[lx], 40960\n\t"
    "s_add_u32 %[t1], %[t0], 0\n\t"
    "buffer_load_dwordx4 %[vw], %[srdw], %[t1] offen lds\n\t"
    "s_add_u32 m0, %[lx], 41984\n\t"
    "s_add_u32 %[t1], %[t0], 21504\n\t"
    "buffer_load_dwordx4 %[vw], %[srdw], %[t1] offen lds\n\t"
    "s_add_u32 m0, %[lx], 49152\n\t"
    "s_add_u32 %[t1], %[t0], 896\n\t"
    "buffer_load_dwordx4 %[vw], %[srdw], %[t1] offen lds\n\t"
    "s_add_u32 m0, %[lx], 50176\n\t"
    "s_add_u32 %[t1], %[t0], 22400\n\t"
    "buffer_load_dwordx4 %[vw], %[srdw], %[t1] offen lds\n\t"
    "s_add_u32 m0, %[lx], 57344\n\t"
    "s_add_u32 %[t1], %[t0], 1792\n\t"
    "buffer_load_dwordx4 %[vw], %[srdw], %[t1] offen lds\n\t"
    "s_add_u32 m0, %[lx], 58368\n\t"
    "s_add_u32 %[t1], %[t0], 23296\n\t"
    "buffer_load_dwordx4 %[vw], %[srdw], %[t1] offen lds\n\t"
    "s_add_u32 %[kx], %[kx], 128\n\t"
    "s_cmp_eq_u32 %[kx], 896\n\t"
    "s_cselect_b32 %[kx], 0, %[kx]\n\t"
    "s_barrier\n\t"
    "s_add_u32 m0, %[lx], 65536\n\t"
    "s_mul_i32 %[t0], %[kx], 1344\n\t"
    "buffer_load_dwordx4 %[vxe], %[srdx], %[kx] offen lds\n\t"
    "s_add_u32 m0, %[lx], 66560\n\t"
    "s_nop 0\n\t"
    "buffer_load_dwordx4 %[vxo], %[srdx], %[kx] offen lds\n\t"
    "s_add_u32 m0, %[lx], 73728\n\t"
    "s_add_u32 %[t1], %[t0], 0\n\t"
    "buffer_load_dwordx4 %[vw], %[srdw], %[t1] offen lds\n\t"
    "s_add_u32 m0, %[lx], 74752\n\t"
    "s_add_u32 %[t1], %[t0], 21504\n\t"
    "buffer_load_dwordx4 %[vw], %[srdw], %[t1] offen lds\n\t"
    "s_add_u32 m0, %[lx], 81920\n\t"
    "s_add_u32 %[t1], %[t0], 896\n\t"
    "buffer_load_dwordx4 %[vw], %[srdw], %[t1] offen lds\n\t"
    "s_add_u32 m0, %[lx], 82944\n\t"
    "s_add_u32 %[t1], %[t0], 22400\n\t"
    "buffer_load_dwordx4 %[vw], %[srdw], %[t1] offen lds\n\t"
    "s_add_u32 m0, %[lx], 90112\n\t"
    "s_add_u32 %[t1], %[t0], 1792\n\t"
    "buffer_load_dwordx4 %[vw], %[srdw], %[t1] offen lds\n\t"
    "s_add_u32 m0, %[lx], 91136\n\t"
    "s_add_u32 %[t1], %[t0], 23296\n\t"
    "buffer_load_dwordx4 %[vw], %[srdw], %[t1] offen lds\n\t"
    "s_add_u32 %[kx], %[kx], 128\n\t"
    "s_cmp_eq_u32 %[kx], 896\n\t"
    "s_cselect_b32 %[kx], 0, %[kx]\n\t"
    "s_waitcnt vmcnt(16)\n\t"
    "s_barrier\n\t"
    "s_add_u32 m0, %[lx], 98304\n\t"
    "s_mul_i32 %[t0], %[kx], 1344\n\t"
    "buffer_load_dwordx4 %[vxe], %[srdx], %[kx] offen lds\n\t"
    "s_add_u32 m0, %[lx], 99328\n\t"
    "s_nop 0\n\t"
    "buffer_load_dwordx4 %[vxo], %[srdx], %[kx] offen lds\n\t"
    "s_add_u32 m0, %[lx], 106496\n\t"
    "s_add_u32 %[t1], %[t0], 0\n\t"
    "buffer_load_dwordx4 %[vw], %[srdw], %[t1] offen lds\n\t"
    "s_add_u32 m0, %[lx], 107520\n\t"
    "s_add_u32 %[t1], %[t0], 21504\n\t"
    "buffer_load_dwordx4 %[vw], %[srdw], %[t1] offen lds\n\t"
    "s_add_u32 m0, %[lx], 114688\n\t"
    "s_add_u32 %[t1], %[t0], 896\n\t"
    "buffer_load_dwordx4 %[vw], %[srdw], %[t1] offen lds\n\t"
    "s_add_u32 m0, %[lx], 115712\n\t"
    "s_add_u32 %[t1], %[t0], 22400\n\t"
    "buffer_load_dwordx4 %[vw], %[srdw], %[t1] offen lds\n\t"
    "s_add_u32 m0, %[lx], 122880\n\t"
    "s_add_u32 %[t1], %[t0], 1792\n\t"
    "buffer_load_dwordx4 %[vw], %[srdw], %[t1] offen lds\n\t"
    "s_add_u32 m0, %[lx], 123904\n\t"
    "s_add_u32 %[t1], %[t0], 23296\n\t"
    "buffer_load_dwordx4 %[vw], %[srdw], %[t1] offen lds\n\t"
    "s_add_u32 %[kx], %[kx], 128\n\t"
    "s_cmp_eq_u32 %[kx], 896\n\t"
    "s_cselect_b32 %[kx], 0, %[kx]\n\t"
    "s_waitcnt vmcnt(16)\n\t"
    "s_barrier\n\t"
    "s_add_u32 m0, %[lx], 0\n\t"
    "s_mul_i32 %[t0], %[kx], 1344\n\t"
    "buffer_load_dwordx4 %[vxe], %[srdx], %[kx] offen lds\n\t"
    "s_add_u32 m0, %[lx], 1024\n\t"
    "s_nop 0\n\t"
    "buffer_load_dwordx4 %[vxo], %[srdx], %[kx] offen lds\n\t"
    "s_add_u32 m0, %[lx], 8192\n\t"
    "s_add_u32 %[t1], %[t0], 0\n\t"
    "buffer_load_dwordx4 %[vw], %[srdw], %[t1] offen lds\n\t"
    "s_add_u32 m0, %[lx], 9216\n\t"
    "s_add_u32 %[t1], %[t0], 21504\n\t"
    "buffer_load_dwordx4 %[vw], %[srdw], %[t1] offen lds\n\t"
    "s_add_u32 m0, %[lx], 16384\n\t"
    "s_add_u32 %[t1], %[t0], 896\n\t"
    "buffer_load_dwordx4 %[vw], %[srdw], %[t1] offen lds\n\t"
    "s_add_u32 m0, %[lx], 17408\n\t"
    "s_add_u32 %[t1], %[t0], 22400\n\t"
    "buffer_load_dwordx4 %[vw], %[srdw], %[t1] offen lds\n\t"
    "s_add_u32 m0, %[lx], 24576\n\t"
    "s_add_u32 %[t1], %[t0], 1792\n\t"
    "buffer_load_dwordx4 %[vw], %[srdw], %[t1] offen lds\n\t"
    "s_add_u32 m0, %[lx], 25600\n\t"
    "s_add_u32 %[t1], %[t0], 23296\n\t"
    "buffer_load_dwordx4 %[vw], %[srdw], %[t1] offen lds\n\t"
    "s_add_u32 %[kx], %[kx], 128\n\t"
    "s_cmp_eq_u32 %[kx], 896\n\t"
    "s_cselect_b32 %[kx], 0, %[kx]\n\t"
    "s_waitcnt vmcnt(16)\n\t"
    "s_barrier\n\t"
    "s_add_u32 m0, %[lx], 32768\n\t"
    "s_mul_i32 %[t0], %[kx], 1344\n\t"
    "buffer_load_dwordx4 %[vxe], %[srdx], %[kx] offen lds\n\t"
    "s_add_u32 m0, %[lx], 33792\n\t"
    "s_nop 0\n\t"
    "buffer_load_dwordx4 %[vxo], %[srdx], %[kx] offen lds\n\t"
    "s_add_u32 m0, %[lx], 40960\n\t"
    "s_add_u32 %[t1], %[t0], 0\n\t"
    "buffer_load_dwordx4 %[vw], %[srdw], %[t1] offen lds\n\t"
    "s_add_u32 m0, %[lx], 41984\n\t"
    "s_add_u32 %[t1], %[t0], 21504\n\t"
    "buffer_load_dwordx4 %[vw], %[srdw], %[t1] offen lds\n\t"
    "s_add_u32 m0, %[lx], 49152\n\t"
    "s_add_u32 %[t1], %[t0], 896\n\t"
    "buffer_load_dwordx4 %[vw], %[srdw], %[t1] offen lds\n\t"
    "s_add_u32 m0, %[lx], 50176\n\t"
    "s_add_u32 %[t1], %[t0], 22400\n\t"
    "buffer_load_dwordx4 %[vw], %[srdw], %[t1] offen lds\n\t"
    "s_add_u32 m0, %[lx], 57344\n\t"
    "s_add_u32 %[t1], %[t0], 1792\n\t"
    "buffer_load_dwordx4 %[vw], %[srdw], %[t1] offen lds\n\t"
    "s_add_u32 m0, %[lx], 58368\n\t"
    "s_add_u32 %[t1], %[t0], 23296\n\t"
    "buffer_load_dwordx4 %[vw], %[srdw], %[t1] offen lds\n\t"
    "s_add_u32 %[kx], %[kx], 128\n\t"
    "s_cmp_eq_u32 %[kx], 896\n\t"
    "s_cselect_b32 %[kx], 0, %[kx]\n\t"
    "s_waitcnt vmcnt(16)\n\t"
    "s_barrier\n\t"
    "s_add_u32 m0, %[lx], 65536\n\t"
    "s_mul_i32 %[t0], %[kx], 1344\n\t"
    "buffer_load_dwordx4 %[vxe], %[srdx], %[kx] offen lds\n\t"
    "s_add_u32 m0, %[lx], 66560\n\t"
    "s_nop 0\n\t"
    "buffer_load_dwordx4 %[vxo], %[srdx], %[kx] offen lds\n\t"
    "s_add_u32 m0, %[lx], 73728\n\t"
    "s_add_u32 %[t1], %[t0], 0\n\t"
    "buffer_load_dwordx4 %[vw], %[srdw], %[t1] offen lds\n\t"
    "s_add_u32 m0, %[lx], 74752\n\t"
    "s_add_u32 %[t1], %[t0], 21504\n\t"
    "buffer_load_dwordx4 %[vw], %[srdw], %[t1] offen lds\n\t"
    "s_add_u32 m0, %[lx], 81920\n\t"
    "s_add_u32 %[t1], %[t0], 896\n\t"
    "buffer_load_dwordx4 %[vw], %[srdw], %[t1] offen lds\n\t"
    "s_add_u32 m0, %[lx], 82944\n\t"
    "s_add_u32 %[t1], %[t0], 22400\n\t"
    "buffer_load_dwordx4 %[vw], %[srdw], %[t1] offen lds\n\t"
    "s_add_u32 m0, %[lx], 90112\n\t"
    "s_add_u32 %[t1], %[t0], 1792\n\t"
    "buffer_load_dwordx4 %[vw], %[srdw], %[t1] offen lds\n\t"
    "s_add_u32 m0, %[lx], 91136\n\t"
    "s_add_u32 %[t1], %[t0], 23296\n\t"
    "buffer_load_dwordx4 %[vw], %[srdw], %[t1] offen lds\n\t"
    "s_add_u32 %[kx], %[kx], 128\n\t"
    "s_cmp_eq_u32 %[kx], 896\n\t"
    "s_cselect_b32 %[kx], 0, %[kx]\n\t"
    "s_waitcnt vmcnt(16)\n\t"
    "s_barrier\n\t"
    "s_waitcnt vmcnt(8)\n\t"
    "s_barrier\n\t"
    "s_waitcnt vmcnt(0)\n\t"
    "s_barrier\n\t"
    "s_mov_b32 m0, %[m0s]\n\t"
                : [t0] "=&s"(t0), [t1] "=&s"(t1), [m0s] "=&s"(m0s), [kx] "+s"(kx)
                : [vxe] "v"(vxe), [vxo] "v"(vxo), [vw] "v"(vw),
                  [srdx] "s"(srdX), [srdw] "s"(srdW), [lx] "s"(lx)
                : "memory", "scc");
        }

        cpx lam, coef;
        {
            const float lr = bf2f((u16)lamw), li = bf2f((u16)lamw2);
            const float dt = rbf(__builtin_amdgcn_exp2f(bf2f((u16)dtw) * 1.4426950408889634f));
            const float a = dt * lr, bb = dt * li;
            const float ea = __builtin_amdgcn_exp2f(a * 1.4426950408889634f);
            float rev = bb * 0.15915494309189535f; rev -= __builtin_floorf(rev);
            lam.re = ea * __builtin_amdgcn_cosf(rev); lam.im = ea * __builtin_amdgcn_sinf(rev);
            const float nr = lam.re - 1.f, ni = lam.im, den = 2.f / (lr * lr + li * li);
            coef.re = (nr * lr + ni * li) * den; coef.im = (ni * lr - nr * li) * den;
        }
        cpx l16, l128, pw;
        {
            const cpx l2 = cmul(lam, lam), l4 = cmul(l2, l2), l8 = cmul(l4, l4);
            l16 = cmul(l8, l8);
            const cpx l32 = cmul(l16, l16), l64 = cmul(l32, l32);
            l128 = cmul(l64, l64);
            pw = hi ? l16 : (cpx){1.f, 0.f};
            if (sw & 1) { UNIFORM_BRANCH(); pw = cmul(pw, l32); }
            if (sw & 2) { UNIFORM_BRANCH(); pw = cmul(pw, l64); }
        }
        asm volatile("" : "+v"(lam.re), "+v"(lam.im), "+v"(coef.re), "+v"(coef.im), "+v"(l16.re), "+v"(l16.im), "+v"(l128.re), "+v"(l128.im), "+v"(pw.re), "+v"(pw.im));
        MAKE_TAG();
        asm volatile("s_barrier" ::: "memory");

        __builtin_amdgcn_s_setprio(2);
        f32x16 ur, ui;
        {
            const int trow = 32 * sw + 16 * ((r32 >> 2) & 1) + (r32 & 3) + 4 * (r32 >> 3);
            const unsigned aoff = ldsb + trow * 128, asw = (trow >> 1) & 3, boff = ldsb + 16384 + r32 * 128, bsw = (r32 >> 1) & 3;
            const unsigned ax0 = aoff + (((0 + hi) ^ asw) << 4), ax1 = aoff + (((2 + hi) ^ asw) << 4), ax2 = aoff + (((4 + hi) ^ asw) << 4), ax3 = aoff + (((6 + hi) ^ asw) << 4);
            const unsigned ab0 = boff + (((0 + hi) ^ bsw) << 4), ab1 = boff + (((2 + hi) ^ bsw) << 4), ab2 = boff + (((4 + hi) ^ bsw) << 4), ab3 = boff + (((6 + hi) ^ bsw) << 4);
            unsigned t1, m0s;
            asm volatile(
    "s_mov_b32 %[m0s], m0\n\t"
    "s_add_u32 %[t1], %[ks], 14336\n\t"
    "s_add_u32 m0, %[la], 0\n\t"
    "s_nop 0\n\t"
    "buffer_load_dwordx4 %[axe], %[srda], %[ks] offen lds\n\t"
    "s_add_u32 m0, %[la], 1024\n\t"
    "s_nop 0\n\t"
    "buffer_load_dwordx4 %[axo], %[srda], %[ks] offen lds\n\t"
    "s_add_u32 m0, %[la], 2048\n\t"
    "s_nop 0\n\t"
    "buffer_load_dwordx4 %[axe], %[srda], %[t1] offen lds\n\t"
    "s_add_u32 m0, %[la], 3072\n\t"
    "s_nop 0\n\t"
    "buffer_load_dwordx4 %[axo], %[srda], %[t1] offen lds\n\t"
    "s_add_u32 m0, %[lb], 0\n\t"
    "s_nop 0\n\t"
    "buffer_load_dwordx4 %[vb], %[srdr], %[ks] offen lds\n\t"
    "s_add_u32 m0, %[lb], 4096\n\t"
    "s_nop 0\n\t"
    "buffer_load_dwordx4 %[vb], %[srdi], %[ks] offen lds\n\t"
    "s_add_u32 %[ks], %[ks], 128\n\t"
    "s_cmp_eq_u32 %[ks], 896\n\t"
    "s_cselect_b32 %[ks], 0, %[ks]\n\t"
    "s_add_u32 %[t1], %[ks], 14336\n\t"
    "s_add_u32 m0, %[la], 24576\n\t"
    "s_nop 0\n\t"
    "buffer_load_dwordx4 %[axe], %[srda], %[ks] offen lds\n\t"
    "s_add_u32 m0, %[la], 25600\n\t"
    "s_nop 0\n\t"
    "buffer_load_dwordx4 %[axo], %[srda], %[ks] offen lds\n\t"
    "s_add_u32 m0, %[la], 26624\n\t"
    "s_nop 0\n\t"
    "buffer_load_dwordx4 %[axe], %[srda], %[t1] offen lds\n\t"
    "s_add_u32 m0, %[la], 27648\n\t"
    "s_nop 0\n\t"
    "buffer_load_dwordx4 %[axo], %[srda], %[t1] offen lds\n\t"
    "s_add_u32 m0, %[lb], 24576\n\t"
    "s_nop 0\n\t"
    "buffer_load_dwordx4 %[vb], %[srdr], %[ks] offen lds\n\t"
    "s_add_u32 m0, %[lb], 28672\n\t"
    "s_nop 0\n\t"
    "buffer_load_dwordx4 %[vb], %[srdi], %[ks] offen lds\n\t"
    "s_add_u32 %[ks], %[ks], 128\n\t"
    "s_cmp_eq_u32 %[ks], 896\n\t"
    "s_cselect_b32 %[ks], 0, %[ks]\n\t"
    "v_mov_b32 v144, %[ax0]\n\t"
    "v_mov_b32 v148, %[ab0]\n\t"
    "v_add_u32 v132, 49152, %[ax0]\n\t"
    "v_add_u32 v136, 49152, %[ab0]\n\t"
    "v_mov_b32 v145, %[ax1]\n\t"
    "v_mov_b32 v149, %[ab1]\n\t"
    "v_add_u32 v133, 49152, %[ax1]\n\t"
    "v_add_u32 v137, 49152, %[ab1]\n\t"
    "v_mov_b32 v146, %[ax2]\n\t"
    "v_mov_b32 v150, %[ab2]\n\t"
    "v_add_u32 v134, 49152, %[ax2]\n\t"
    "v_add_u32 v138, 49152, %[ab2]\n\t"
    "v_mov_b32 v147, %[ax3]\n\t"
    "v_mov_b32 v151, %[ab3]\n\t"
    "v_add_u32 v135, 49152, %[ax3]\n\t"
    "v_add_u32 v139, 49152, %[ab3]\n\t"
    "v_mov_b32 v64, 0\n\t"
    "v_mov_b32 v65, 0\n\t"
    "v_mov_b32 v66, 0\n\t"
    "v_mov_b32 v67, 0\n\t"
    "v_mov_b32 v68, 0\n\t"
    "v_mov_b32 v69, 0\n\t"
    "v_mov_b32 v70, 0\n\t"
    "v_mov_b32 v71, 0\n\t"
    "v_mov_b32 v72, 0\n\t"
    "v_mov_b32 v73, 0\n\t"
    "v_mov_b32 v74, 0\n\t"
    "v_mov_b32 v75, 0\n\t"
    "v_mov_b32 v76, 0\n\t"
    "v_mov_b32 v77, 0\n\t"
    "v_mov_b32 v78, 0\n\t"
    "v_mov_b32 v79, 0\n\t"
    "v_mov_b32 v80, 0\n\t"
    "v_mov_b32 v81, 0\n\t"
    "v_mov_b32 v82, 0\n\t"
    "v_mov_b32 v83, 0\n\t"
    "v_mov_b32 v84, 0\n\t"
    "v_mov_b32 v85, 0\n\t"
    "v_mov_b32 v86, 0\n\t"
    "v_mov_b32 v87, 0\n\t"
    "v_mov_b32 v88, 0\n\t"
    "v_mov_b32 v89, 0\n\t"
    "v_mov_b32 v90, 0\n\t"
    "v_mov_b32 v91, 0\n\t"
    "v_mov_b32 v92, 0\n\t"
    "v_mov_b32 v93, 0\n\t"
    "v_mov_b32 v94, 0\n\t"
    "v_mov_b32 v95, 0\n\t"
    "s_waitcnt vmcnt(0)\n\t"
    "s_add_u32 %[ep], %[ep], 1\n\t"
    "v_mov_b32 v152, %[ep]\n\t"
    "ds_write_b32 %[cntw], v152\n\t"
    "11:\n\t"
    "ds_read_b32 v152, %[cntr]\n\t"
    "s_waitcnt lgkmcnt(0)\n\t"
    "v_cmp_gt_u32 vcc, %[ep], v152\n\t"
    "s_cbranch_vccz 41f\n\t"
    "s_sleep 1\n\t"
    "s_branch 11b\n\t"
    "41:\n\t"
    "ds_read_b128 v[96:99], v144 offset:0\n\t"
    "ds_read_b128 v[108:111], v148 offset:0\n\t"
    "ds_read_b128 v[120:123], v148 offset:4096\n\t"
    "ds_read_b128 v[100:103], v145 offset:0\n\t"
    "ds_read_b128 v[112:115], v149 offset:0\n\t"
    "ds_read_b128 v[124:127], v149 offset:4096\n\t"
    "s_waitcnt lgkmcnt(3)\n\t"
    "v_mfma_f32_32x32x16_bf16 v[64:79], v[96:99], v[108:111], v[64:79]\n\t"
    "v_mfma_f32_32x32x16_bf16 v[80:95], v[96:99], v[120:123], v[80:95]\n\t"
    "ds_read_b128 v[104:107], v146 offset:0\n\t"
    "ds_read_b128 v[116:119], v150 offset:0\n\t"
    "ds_read_b128 v[128:131], v150 offset:4096\n\t"
    "s_add_u32 %[t1], %[ks], 14336\n\t"
    "s_add_u32 m0, %[la], 49152\n\t"
    "s_nop 0\n\t"
    "buffer_load_dwordx4 %[axe], %[srda], %[ks] offen lds\n\t"
    "s_add_u32 m0, %[la], 50176\n\t"
    "s_nop 0\n\t"
    "buffer_load_dwordx4 %[axo], %[srda], %[ks] offen lds\n\t"
    "s_add_u32 m0, %[la], 51200\n\t"
    "s_nop 0\n\t"
    "buffer_load_dwordx4 %[axe], %[srda], %[t1] offen lds\n\t"
    "s_waitcnt lgkmcnt(3)\n\t"
    "v_mfma_f32_32x32x16_bf16 v[64:79], v[100:103], v[112:115], v[64:79]\n\t"
    "v_mfma_f32_32x32x16_bf16 v[80:95], v[100:103], v[124:127], v[80:95]\n\t"
    "ds_read_b128 v[96:99], v147 offset:0\n\t"
    "ds_read_b128 v[108:111], v151 offset:0\n\t"
    "ds_read_b128 v[120:123], v151 offset:4096\n\t"
    "s_add_u32 m0, %[la], 52224\n\t"
    "s_nop 0\n\t"
    "buffer_load_dwordx4 %[axo], %[srda], %[t1] offen lds\n\t"
    "s_add_u32 m0, %[lb], 49152\n\t"
    "s_nop 0\n\t"
    "buffer_load_dwordx4 %[vb], %[srdr], %[ks] offen lds\n\t"
    "s_add_u32 m0, %[lb], 53248\n\t"
    "s_nop 0\n\t"
    "buffer_load_dwordx4 %[vb], %[srdi], %[ks] offen lds\n\t"
    "s_add_u32 %[ks], %[ks], 128\n\t"
    "s_cmp_eq_u32 %[ks], 896\n\t"
    "s_cselect_b32 %[ks], 0, %[ks]\n\t"
    "s_waitcnt lgkmcnt(3)\n\t"
    "v_mfma_f32_32x32x16_bf16 v[64:79], v[104:107], v[116:119], v[64:79]\n\t"
    "v_mfma_f32_32x32x16_bf16 v[80:95], v[104:107], v[128:131], v[80:95]\n\t"
    "ds_read_b128 v[100:103], v144 offset:24576\n\t"
    "ds_read_b128 v[112:115], v148 offset:24576\n\t"
    "ds_read_b128 v[124:127], v148 offset:28672\n\t"
    "s_add_u32 %[t1], %[ks], 14336\n\t"
    "s_add_u32 m0, %[la], 73728\n\t"
    "s_nop 0\n\t"
    "buffer_load_dwordx4 %[axe], %[srda], %[ks] offen lds\n\t"
    "s_add_u32 m0, %[la], 74752\n\t"
    "s_nop 0\n\t"
    "buffer_load_dwordx4 %[axo], %[srda], %[ks] offen lds\n\t"
    "s_add_u32 m0, %[la], 75776\n\t"
    "s_nop 0\n\t"
    "buffer_load_dwordx4 %[axe], %[srda], %[t1] offen lds\n\t"
    "s_waitcnt lgkmcnt(3)\n\t"
    "v_mfma_f32_32x32x16_bf16 v[64:79], v[96:99], v[108:111], v[64:79]\n\t"
    "v_mfma_f32_32x32x16_bf16 v[80:95], v[96:99], v[120:123], v[80:95]\n\t"
    "ds_read_b128 v[104:107], v145 offset:24576\n\t"
    "ds_read_b128 v[116:119], v149 offset:24576\n\t"
    "ds_read_b128 v[128:131], v149 offset:28672\n\t"
    "s_add_u32 m0, %[la], 76800\n\t"
    "s_nop 0\n\t"
    "buffer_load_dwordx4 %[axo], %[srda], %[t1] offen lds\n\t"
    "s_add_u32 m0, %[lb], 73728\n\t"
    "s_nop 0\n\t"
    "buffer_load_dwordx4 %[vb], %[srdr], %[ks] offen lds\n\t"
    "s_add_u32 m0, %[lb], 77824\n\t"
    "s_nop 0\n\t"
    "buffer_load_dwordx4 %[vb], %[srdi], %[ks] offen lds\n\t"
    "s_add_u32 %[ks], %[ks], 128\n\t"
    "s_cmp_eq_u32 %[ks], 896\n\t"
    "s_cselect_b32 %[ks], 0, %[ks]\n\t"
    "s_waitcnt lgkmcnt(3)\n\t"
    "v_mfma_f32_32x32x16_bf16 v[64:79], v[100:103], v[112:115], v[64:79]\n\t"
    "v_mfma_f32_32x32x16_bf16 v[80:95], v[100:103], v[124:127], v[80:95]\n\t"
    "ds_read_b128 v[96:99], v146 offset:24576\n\t"
    "ds_read_b128 v[108:111], v150 offset:24576\n\t"
    "ds_read_b128 v[120:123], v150 offset:28672\n\t"
    "s_add_u32 %[t1], %[ks], 14336\n\t"
    "s_add_u32 m0, %[la], 98304\n\t"
    "s_nop 0\n\t"
    "buffer_load_dwordx4 %[axe], %[srda], %[ks] offen lds\n\t"
    "s_add_u32 m0, %[la], 99328\n\t"
    "s_nop 0\n\t"
    "buffer_load_dwordx4 %[axo], %[srda], %[ks] offen lds\n\t"
    "s_add_u32 m0, %[la], 100352\n\t"
    "s_nop 0\n\t"
    "buffer_load_dwordx4 %[axe], %[srda], %[t1] offen lds\n\t"
    "s_waitcnt lgkmcnt(3)\n\t"
    "v_mfma_f32_32x32x16_bf16 v[64:79], v[104:107], v[116:119], v[64:79]\n\t"
    "v_mfma_f32_32x32x16_bf16 v[80:95], v[104:107], v[128:131], v[80:95]\n\t"
    "ds_read_b128 v[100:103], v147 offset:24576\n\t"
    "ds_read_b128 v[112:115], v151 offset:24576\n\t"
    "ds_read_b128 v[124:127], v151 offset:28672\n\t"
    "s_add_u32 m0, %[la], 101376\n\t"
    "s_nop 0\n\t"
    "buffer_load_dwordx4 %[axo], %[srda], %[t1] offen lds\n\t"
    "s_add_u32 m0, %[lb], 98304\n\t"
    "s_nop 0\n\t"
    "buffer_load_dwordx4 %[vb], %[srdr], %[ks] offen lds\n\t"
    "s_add_u32 m0, %[lb], 102400\n\t"
    "s_nop 0\n\t"
    "buffer_load_dwordx4 %[vb], %[srdi], %[ks] offen lds\n\t"
    "s_add_u32 %[ks], %[ks], 128\n\t"
    "s_cmp_eq_u32 %[ks], 896\n\t"
    "s_cselect_b32 %[ks], 0, %[ks]\n\t"
    "s_waitcnt lgkmcnt(3)\n\t"
    "v_mfma_f32_32x32x16_bf16 v[64:79], v[96:99], v[108:111], v[64:79]\n\t"
    "v_mfma_f32_32x32x16_bf16 v[80:95], v[96:99], v[120:123], v[80:95]\n\t"
    "s_waitcnt lgkmcnt(0)\n\t"
    "v_mfma_f32_32x32x16_bf16 v[64:79], v[100:103], v[112:115], v[64:79]\n\t"
    "v_mfma_f32_32x32x16_bf16 v[80:95], v[100:103], v[124:127], v[80:95]\n\t"
    "s_waitcnt vmcnt(0)\n\t"
    "s_add_u32 %[ep], %[ep], 1\n\t"
    "v_mov_b32 v152, %[ep]\n\t"
    "ds_write_b32 %[cntw], v152\n\t"
    "12:\n\t"
    "ds_read_b32 v152, %[cntr]\n\t"
    "s_waitcnt lgkmcnt(0)\n\t"
    "v_cmp_gt_u32 vcc, %[ep], v152\n\t"
    "s_cbranch_vccz 42f\n\t"
    "s_sleep 1\n\t"
    "s_branch 12b\n\t"
    "42:\n\t"
    "ds_read_b128 v[104:107], v132 offset:0\n\t"
    "ds_read_b128 v[116:119], v136 offset:0\n\t"
    "ds_read_b128 v[128:131], v136 offset:4096\n\t"
    "ds_read_b128 v[96:99], v133 offset:0\n\t"
    "ds_read_b128 v[108:111], v137 offset:0\n\t"
    "ds_read_b128 v[120:123], v137 offset:4096\n\t"
    "s_waitcnt lgkmcnt(3)\n\t"
    "v_mfma_f32_32x32x16_bf16 v[64:79], v[104:107], v[116:119], v[64:79]\n\t"
    "v_mfma_f32_32x32x16_bf16 v[80:95], v[104:107], v[128:131], v[80:95]\n\t"
    "ds_read_b128 v[100:103], v134 offset:0\n\t"
    "ds_read_b128 v[112:115], v138 offset:0\n\t"
    "ds_read_b128 v[124:127], v138 offset:4096\n\t"
    "s_add_u32 %[t1], %[ks], 14336\n\t"
    "s_add_u32 m0, %[la], 0\n\t"
    "s_nop 0\n\t"
    "buffer_load_dwordx4 %[axe], %[srda], %[ks] offen lds\n\t"
    "s_waitcnt lgkmcnt(3)\n\t"
    "v_mfma_f32_32x32x16_bf16 v[64:79], v[96:99], v[108:111], v[64:79]\n\t"
    "v_mfma_f32_32x32x16_bf16 v[80:95], v[96:99], v[120:123], v[80:95]\n\t"
    "ds_read_b128 v[104:107], v135 offset:0\n\t"
    "ds_read_b128 v[116:119], v139 offset:0\n\t"
    "ds_read_b128 v[128:131], v139 offset:4096\n\t"
    "s_add_u32 m0, %[la], 1024\n\t"
    "s_nop 0\n\t"
    "buffer_load_dwordx4 %[axo], %[srda], %[ks] offen lds\n\t"
    "s_waitcnt lgkmcnt(3)\n\t"
    "v_mfma_f32_32x32x16_bf16 v[64:79], v[100:103], v[112:115], v[64:79]\n\t"
    "v_mfma_f32_32x32x16_bf16 v[80:95], v[100:103], v[124:127], v[80:95]\n\t"
    "ds_read_b128 v[96:99], v132 offset:24576\n\t"
    "ds_read_b128 v[108:111], v136 offset:24576\n\t"
    "ds_read_b128 v[120:123], v136 offset:28672\n\t"
    "s_add_u32 m0, %[la], 2048\n\t"
    "s_nop 0\n\t"
    "buffer_load_dwordx4 %[axe], %[srda], %[t1] offen lds\n\t"
    "s_waitcnt lgkmcnt(3)\n\t"
    "v_mfma_f32_32x32x16_bf16 v[64:79], v[104:107], v[116:119], v[64:79]\n\t"
    "v_mfma_f32_32x32x16_bf16 v[80:95], v[104:107], v[128:131], v[80:95]\n\t"
    "ds_read_b128 v[100:103], v133 offset:24576\n\t"
    "ds_read_b128 v[112:115], v137 offset:24576\n\t"
    "ds_read_b128 v[124:127], v137 offset:28672\n\t"
    "s_add_u32 m0, %[la], 3072\n\t"
    "s_nop 0\n\t"
    "buffer_load_dwordx4 %[axo], %[srda], %[t1] offen lds\n\t"
    "s_waitcnt lgkmcnt(3)\n\t"
    "v_mfma_f32_32x32x16_bf16 v[64:79], v[96:99], v[108:111], v[64:79]\n\t"
    "v_mfma_f32_32x32x16_bf16 v[80:95], v[96:99], v[120:123], v[80:95]\n\t"
    "ds_read_b128 v[104:107], v134 offset:24576\n\t"
    "ds_read_b128 v[116:119], v138 offset:24576\n\t"
    "ds_read_b128 v[128:131], v138 offset:28672\n\t"
    "s_add_u32 m0, %[lb], 0\n\t"
    "s_nop 0\n\t"
    "buffer_load_dwordx4 %[vb], %[srdr], %[ks] offen lds\n\t"
    "s_waitcnt lgkmcnt(3)\n\t"
    "v_mfma_f32_32x32x16_bf16 v[64:79], v[100:103], v[112:115], v[64:79]\n\t"
    "v_mfma_f32_32x32x16_bf16 v[80:95], v[100:103], v[124:127], v[80:95]\n\t"
    "ds_read_b128 v[96:99], v135 offset:24576\n\t"
    "ds_read_b128 v[108:111], v139 offset:24576\n\t"
    "ds_read_b128 v[120:123], v139 offset:28672\n\t"
    "s_add_u32 m0, %[lb], 4096\n\t"
    "s_nop 0\n\t"
    "buffer_load_dwordx4 %[vb], %[srdi], %[ks] offen lds\n\t"
    "s_add_u32 %[ks], %[ks], 128\n\t"
    "s_cmp_eq_u32 %[ks], 896\n\t"
    "s_cselect_b32 %[ks], 0, %[ks]\n\t"
    "s_waitcnt lgkmcnt(3)\n\t"
    "v_mfma_f32_32x32x16_bf16 v[64:79], v[104:107], v[116:119], v[64:79]\n\t"
    "v_mfma_f32_32x32x16_bf16 v[80:95], v[104:107], v[128:131], v[80:95]\n\t"
    "ds_read_b128 v[100:103], v132 offset:49152\n\t"
    "ds_read_b128 v[112:115], v136 offset:49152\n\t"
    "ds_read_b128 v[124:127], v136 offset:53248\n\t"
    "s_add_u32 %[t1], %[ks], 14336\n\t"
    "s_add_u32 m0, %[la], 24576\n\t"
    "s_nop 0\n\t"
    "buffer_load_dwordx4 %[axe], %[srda], %[ks] offen lds\n\t"
    "s_waitcnt lgkmcnt(3)\n\t"
    "v_mfma_f32_32x32x16_bf16 v[64:79], v[96:99], v[108:111], v[64:79]\n\t"
    "v_mfma_f32_32x32x16_bf16 v[80:95], v[96:99], v[120:123], v[80:95]\n\t"
    "ds_read_b128 v[104:107], v133 offset:49152\n\t"
    "ds_read_b128 v[116:119], v137 offset:49152\n\t"
    "ds_read_b128 v[128:131], v137 offset:53248\n\t"
    "s_add_u32 m0, %[la], 25600\n\t"
    "s_nop 0\n\t"
    "buffer_load_dwordx4 %[axo], %[srda], %[ks] offen lds\n\t"
    "s_waitcnt lgkmcnt(3)\n\t"
    "v_mfma_f32_32x32x16_bf16 v[64:79], v[100:103], v[112:115], v[64:79]\n\t"
    "v_mfma_f32_32x32x16_bf16 v[80:95], v[100:103], v[124:127], v[80:95]\n\t"
    "ds_read_b128 v[96:99], v134 offset:49152\n\t"
    "ds_read_b128 v[108:111], v138 offset:49152\n\t"
    "ds_read_b128 v[120:123], v138 offset:53248\n\t"
    "s_add_u32 m0, %[la], 26624\n\t"
    "s_nop 0\n\t"
    "buffer_load_dwordx4 %[axe], %[srda], %[t1] offen lds\n\t"
    "s_waitcnt lgkmcnt(3)\n\t"
    "v_mfma_f32_32x32x16_bf16 v[64:79], v[104:107], v[116:119], v[64:79]\n\t"
    "v_mfma_f32_32x32x16_bf16 v[80:95], v[104:107], v[128:131], v[80:95]\n\t"
    "ds_read_b128 v[100:103], v135 offset:49152\n\t"
    "ds_read_b128 v[112:115], v139 offset:49152\n\t"
    "ds_read_b128 v[124:127], v139 offset:53248\n\t"
    "s_add_u32 m0, %[la], 27648\n\t"
    "s_nop 0\n\t"
    "buffer_load_dwordx4 %[axo], %[srda], %[t1] offen lds\n\t"
    "s_waitcnt lgkmcnt(3)\n\t"
    "v_mfma_f32_32x32x16_bf16 v[64:79], v[96:99], v[108:111], v[64:79]\n\t"
    "v_mfma_f32_32x32x16_bf16 v[80:95], v[96:99], v[120:123], v[80:95]\n\t"
    "s_add_u32 m0, %[lb], 24576\n\t"
    "s_nop 0\n\t"
    "buffer_load_dwordx4 %[vb], %[srdr], %[ks] offen lds\n\t"
    "s_waitcnt lgkmcnt(0)\n\t"
    "v_mfma_f32_32x32x16_bf16 v[64:79], v[100:103], v[112:115], v[64:79]\n\t"
    "v_mfma_f32_32x32x16_bf16 v[80:95], v[100:103], v[124:127], v[80:95]\n\t"
    "s_add_u32 m0, %[lb], 28672\n\t"
    "s_nop 0\n\t"
    "buffer_load_dwordx4 %[vb], %[srdi], %[ks] offen lds\n\t"
    "s_add_u32 %[ks], %[ks], 128\n\t"
    "s_cmp_eq_u32 %[ks], 896\n\t"
    "s_cselect_b32 %[ks], 0, %[ks]\n\t"
    "s_waitcnt vmcnt(0)\n\t"
    "s_add_u32 %[ep], %[ep], 1\n\t"
    "v_mov_b32 v152, %[ep]\n\t"
    "ds_write_b32 %[cntw], v152\n\t"
    "13:\n\t"
    "ds_read_b32 v152, %[cntr]\n\t"
    "s_waitcnt lgkmcnt(0)\n\t"
    "v_cmp_gt_u32 vcc, %[ep], v152\n\t"
    "s_cbranch_vccz 43f\n\t"
    "s_sleep 1\n\t"
    "s_branch 13b\n\t"
    "43:\n\t"
    "ds_read_b128 v[104:107], v144 offset:0\n\t"
    "ds_read_b128 v[116:119], v148 offset:0\n\t"
    "ds_read_b128 v[128:131], v148 offset:4096\n\t"
    "ds_read_b128 v[96:99], v145 offset:0\n\t"
    "ds_read_b128 v[108:111], v149 offset:0\n\t"
    "ds_read_b128 v[120:123], v149 offset:4096\n\t"
    "s_waitcnt lgkmcnt(3)\n\t"
    "v_mfma_f32_32x32x16_bf16 v[64:79], v[104:107], v[116:119], v[64:79]\n\t"
    "v_mfma_f32_32x32x16_bf16 v[80:95], v[104:107], v[128:131], v[80:95]\n\t"
    "ds_read_b128 v[100:103], v146 offset:0\n\t"
    "ds_read_b128 v[112:115], v150 offset:0\n\t"
    "ds_read_b128 v[124:127], v150 offset:4096\n\t"
    "s_waitcnt lgkmcnt(3)\n\t"
    "v_mfma_f32_32x32x16_bf16 v[64:79], v[96:99], v[108:111], v[64:79]\n\t"
    "v_mfma_f32_32x32x16_bf16 v[80:95], v[96:99], v[120:123], v[80:95]\n\t"
    "ds_read_b128 v[104:107], v147 offset:0\n\t"
    "ds_read_b128 v[116:119], v151 offset:0\n\t"
    "ds_read_b128 v[128:131], v151 offset:4096\n\t"
    "s_waitcnt lgkmcnt(3)\n\t"
    "v_mfma_f32_32x32x16_bf16 v[64:79], v[100:103], v[112:115], v[64:79]\n\t"
    "v_mfma_f32_32x32x16_bf16 v[80:95], v[100:103], v[124:127], v[80:95]\n\t"
    "ds_read_b128 v[96:99], v144 offset:24576\n\t"
    "ds_read_b128 v[108:111], v148 offset:24576\n\t"
    "ds_read_b128 v[120:123], v148 offset:28672\n\t"
    "s_waitcnt lgkmcnt(3)\n\t"
    "v_mfma_f32_32x32x16_bf16 v[64:79], v[104:107], v[116:119], v[64:79]\n\t"
    "v_mfma_f32_32x32x16_bf16 v[80:95], v[104:107], v[128:131], v[80:95]\n\t"
    "ds_read_b128 v[100:103], v145 offset:24576\n\t"
    "ds_read_b128 v[112:115], v149 offset:24576\n\t"
    "ds_read_b128 v[124:127], v149 offset:28672\n\t"
    "s_waitcnt lgkmcnt(3)\n\t"
    "v_mfma_f32_32x32x16_bf16 v[64:79], v[96:99], v[108:111], v[64:79]\n\t"
    "v_mfma_f32_32x32x16_bf16 v[80:95], v[96:99], v[120:123], v[80:95]\n\t"
    "ds_read_b128 v[104:107], v146 offset:24576\n\t"
    "ds_read_b128 v[116:119], v150 offset:24576\n\t"
    "ds_read_b128 v[128:131], v150 offset:28672\n\t"
    "s_waitcnt lgkmcnt(3)\n\t"
    "v_mfma_f32_32x32x16_bf16 v[64:79], v[100:103], v[112:115], v[64:79]\n\t"
    "v_mfma_f32_32x32x16_bf16 v[80:95], v[100:103], v[124:127], v[80:95]\n\t"
    "ds_read_b128 v[96:99], v147 offset:24576\n\t"
    "ds_read_b128 v[108:111], v151 offset:24576\n\t"
    "ds_read_b128 v[120:123], v151 offset:28672\n\t"
    "s_waitcnt lgkmcnt(3)\n\t"
    "v_mfma_f32_32x32x16_bf16 v[64:79], v[104:107], v[116:119], v[64:79]\n\t"
    "v_mfma_f32_32x32x16_bf16 v[80:95], v[104:107], v[128:131], v[80:95]\n\t"
    "s_waitcnt lgkmcnt(0)\n\t"
    "v_mfma_f32_32x32x16_bf16 v[64:79], v[96:99], v[108:111], v[64:79]\n\t"
    "v_mfma_f32_32x32x16_bf16 v[80:95], v[96:99], v[120:123], v[80:95]\n\t"
    "s_mov_b32 m0, %[m0s]\n\t"
    "s_nop 7\n\t"
    "s_nop 7\n\t"
                : "=&{v[64:79]}"(ur), "=&{v[80:95]}"(ui), [t1] "=&s"(t1), [m0s] "=&s"(m0s), [ep] "+s"(sepoch), [ks] "+s"(ks)
                : [ax0] "v"(ax0), [ax1] "v"(ax1), [ax2] "v"(ax2), [ax3] "v"(ax3), [ab0] "v"(ab0), [ab1] "v"(ab1), [ab2] "v"(ab2), [ab3] "v"(ab3),
                  [axe] "v"(axe), [axo] "v"(axo), [vb] "v"(vb), [cntw] "v"(cntw), [cntr] "v"(cntr),
                  [srda] "s"(srdA), [srdr] "s"(srdR), [srdi] "s"(srdI), [la] "s"(la), [lb] "s"(lb)
                : "memory", "scc", "vcc", "v96", "v97", "v98", "v99", "v100", "v101", "v102", "v103", "v104", "v105", "v106", "v107", "v108", "v109", "v110", "v111", "v112", "v113", "v114", "v115", "v116", "v117", "v118", "v119", "v120", "v121", "v122", "v123", "v124", "v125", "v126", "v127", "v128", "v129", "v130", "v131", "v132", "v133", "v134", "v135", "v136", "v137", "v138", "v139", "v140", "v141", "v142", "v143", "v144", "v145", "v146", "v147", "v148", "v149", "v150", "v151", "v152");
        }
        __builtin_amdgcn_s_setprio(2);

        cpx e = {0.f, 0.f};
#pragma unroll
        for (int i = 0; i < 16; ++i) {
            const float u = ur[i], v = ui[i];
            ur[i] = __builtin_fmaf(coef.re, u, -(coef.im * v)); ui[i] = __builtin_fmaf(coef.re, v, coef.im * u);
            e = cfma(lam, e, (cpx){ur[i], ui[i]});
        }
        *(LAS f32x2*)(lds + L_SEG + ((2 * sw + hi) * 32 + r32) * 8) = (f32x2){e.re, e.im};
        sbar<2>(cntw, cntr, sepoch);
        cpx cseg = {0.f, 0.f};
        {
            f32x2 es[8];
#pragma unroll
            for (int s2 = 0; s2 < 8; ++s2) es[s2] = *(const LAS f32x2*)(lds + L_SEG + (s2 * 32 + r32) * 8);
            if (sw == 0) {
                UNIFORM_BRANCH();
                cpx E = {es[0][0], es[0][1]};
#pragma unroll
                for (int s2 = 1; s2 < 8; ++s2) E = cfma(l16, E, (cpx){es[s2][0], es[s2][1]});
                if (!hi) st16_sc1(wsr, WS_E + ((c * 14 + g) * 32 + r32) * 16, (u32x4){F2U(E.re), F2U(E.im), (unsigned)tag, (unsigned)(tag >> 32)});
                if (hi) cseg = (cpx){es[0][0], es[0][1]};
            } else {
                cseg = cfma(l16, (cpx){es[0][0], es[0][1]}, (cpx){es[1][0], es[1][1]});
                cpx own = {es[2][0], es[2][1]};
                if (sw >= 2) {
                    UNIFORM_BRANCH();
                    cseg = cfma(l16, cfma(l16, cseg, (cpx){es[2][0], es[2][1]}), (cpx){es[3][0], es[3][1]}); own = (cpx){es[4][0], es[4][1]};
                    if (sw == 3) { UNIFORM_BRANCH(); cseg = cfma(l16, cfma(l16, cseg, (cpx){es[4][0], es[4][1]}), (cpx){es[5][0], es[5][1]}); own = (cpx){es[6][0], es[6][1]}; }
                }
                const cpx nx = cfma(l16, cseg, own);
                if (hi) cseg = nx;
            }
        }

        __builtin_amdgcn_s_setprio(0);
        {
            const i32x4 srdCr = make_srd(Cre, DM * DM * 2), srdCi = make_srd(Cim, DM * DM * 2);
            const int row = 16 * sw + (lane >> 3);
            const unsigned vce = (unsigned)((64 * db + row) * 896 + (((lane & 7) ^ ((row >> 1) & 3)) << 4)), vco = (unsigned)((64 * db + row + 8) * 896 + (((lane & 7) ^ (((row + 8) >> 1) & 3)) << 4));
#pragma unroll 1
            for (int p0 = 0; p0 < 7; ++p0) {
                const int pq = p0 + 2 * (idx & 3), pp = pq >= 7 ? pq - 7 : pq;
                const unsigned lc = ldsb + pp * 16384 + sw * 2048;
                dma16(srdCr, vce, pp * 128, lc); dma16(srdCr, vco, pp * 128, lc + 1024); dma16(srdCi, vce, pp * 128, lc + 8192); dma16(srdCi, vco, pp * 128, lc + 9216);
            }
        }
        __builtin_amdgcn_s_setprio(2);
        cpx h = cseg;
        if (c > 0) {
            UNIFORM_BRANCH();
            const unsigned eoff = WS_E + (g * 32 + r32) * 16;
            u32x4 ev[15];
#pragma unroll 1
            for (unsigned spins = 0;; ++spins) {
                const u32x4 w = ld16_sc1(wsr, WS_E + ((lane < c ? lane : 0) * 14 + g) * 512);
                u64 bad = __builtin_amdgcn_ballot_w64((((u64)w[3] << 32) | w[2]) != tag);
                if (bad == 0) {
                    UNIFORM_BRANCH();
#pragma unroll
                    for (int c2 = 0; c2 < 15; ++c2) if (c2 < c) { UNIFORM_BRANCH(); ev[c2] = ld16_sc1(wsr, eoff, c2 * (14 * 32 * 16)); }
#pragma unroll
                    for (int c2 = 0; c2 < 15; ++c2) if (c2 < c) { UNIFORM_BRANCH(); bad |= __builtin_amdgcn_ballot_w64((((u64)ev[c2][3] << 32) | ev[c2][2]) != tag); }
                    if (bad == 0) break;
                }
                if (spins > SPIN_CAP) { __hip_atomic_store(tmo, 1u, RLX_AGENT); break; }
                __builtin_amdgcn_s_sleep(4);
            }
            cpx carry = {U2F(ev[0][0]), U2F(ev[0][1])};
#pragma unroll
            for (int c2 = 1; c2 < 15; ++c2) if (c2 < c) { UNIFORM_BRANCH(); carry = cfma(l128, carry, (cpx){U2F(ev[c2][0]), U2F(ev[c2][1])}); }
            h = cfma(pw, carry, cseg);
        }

#pragma unroll
        for (int i = 0; i < 16; ++i) { h = cfma(lam, h, (cpx){ur[i], ui[i]}); ur[i] = h.re; ui[i] = -h.im; }
        {
            const lptr hb = lds + L_HT + sw * 4096;
            *(LAS bf16x8*)(hb + r32 * 64 + hi * 32) = pack8(ur, 0); *(LAS bf16x8*)(hb + r32 * 64 + hi * 32 + 16) = pack8(ur, 1);
            *(LAS bf16x8*)(hb + 2048 + r32 * 64 + hi * 32) = pack8(ui, 0); *(LAS bf16x8*)(hb + 2048 + r32 * 64 + hi * 32 + 16) = pack8(ui, 1);
            asm volatile("s_waitcnt lgkmcnt(0)" ::: "memory");
            const lptr tp = hb + (8 * hi + ((lane & 15) >> 2)) * 64 + (16 * ((lane >> 4) & 1) + 4 * (lane & 3)) * 2;
            const unsigned hoff = (unsigned)(WS_HF + ((4 * c + sw) * 56 + 4 * g) * 1024 + lane * 16);
#pragma unroll
            for (int f = 0; f < 4; ++f) st16_sc1(wsr, hoff + f * 1024, __builtin_bit_cast(u32x4, lds_tr2(tp + f * 1024, tp + f * 1024 + 256)));
        }
        WAIT_VM(0);
        sbar<2>(cntw, cntr, sepoch);
        if (sw == 0 && lane == 0) __hip_atomic_store(flags + 32 * (F_H + c) + g, tag, RLX_AGENT);
        const int tt = 2 * tb + (sw & 1), kh = sw >> 1;
        const int dcol = 64 * db + 32 * kh + r32;
        unsigned xs[16];
        const unsigned dskw = Dskip[dcol];
        {
            const __amdgpu_buffer_rsrc_t xar = __builtin_amdgcn_make_buffer_rsrc((void*)xA, 0, SEQ * DM * 2, 0x00020000);
            const unsigned xo = (unsigned)(((32 * tt + 4 * hi) * DM + dcol) * 2);
#pragma unroll
            for (int r = 0; r < 16; ++r) xs[r] = __builtin_amdgcn_raw_buffer_load_b16(xar, xo, ((r & 3) + 8 * (r >> 2)) * DM * 2, 0);
        }
        __builtin_amdgcn_s_sleep(16);

        {
            group_wait<2>(flags + 32 * (F_H + (tb >> 1)) + (lane & 15), lane < 14, tag, tmo, sw == 0, lds + L_ACNT + 16);

            f32x16 y0, y1;
            {
                const unsigned hoff = (unsigned)(WS_HF + (tt * 56 + 28 * kh) * 1024 + lane * 16);
                const unsigned csw = (r32 >> 1) & 3, a0 = r32 * 128 + ((hi ^ csw) << 4), a1 = r32 * 128 + (((2 + hi) ^ csw) << 4);
                const unsigned ce0 = ldsb + (kh ? (a0 ^ 64) + 3 * 16384 : a0), ce1 = ldsb + (kh ? (a1 ^ 64) + 3 * 16384 : a1);
                const unsigned co0 = ldsb + (kh ? a0 + 4 * 16384 : (a0 ^ 64)), co1 = ldsb + (kh ? a1 + 4 * 16384 : (a1 ^ 64));
                const i32x4 srdW = make_srd(ws, WS_BYTES);
                unsigned so = 0;
                u32x8 hz[14];
                asm volatile(
    "buffer_load_dwordx4 v[128:131], %[voff], %[srd], %[so] offen offset:0 sc1\n\t"
    "buffer_load_dwordx4 v[132:135], %[voff], %[srd], %[so] offen offset:1024 sc1\n\t"
    "buffer_load_dwordx4 v[136:139], %[voff], %[srd], %[so] offen offset:2048 sc1\n\t"
    "buffer_load_dwordx4 v[140:143], %[voff], %[srd], %[so] offen offset:3072 sc1\n\t"
    "s_add_u32 %[so], %[so], 4096\n\t"
    "buffer_load_dwordx4 v[144:147], %[voff], %[srd], %[so] offen offset:0 sc1\n\t"
    "buffer_load_dwordx4 v[148:151], %[voff], %[srd], %[so] offen offset:1024 sc1\n\t"
    "buffer_load_dwordx4 v[152:155], %[voff], %[srd], %[so] offen offset:2048 sc1\n\t"
    "buffer_load_dwordx4 v[156:159], %[voff], %[srd], %[so] offen offset:3072 sc1\n\t"
    "s_add_u32 %[so], %[so], 4096\n\t"
    "buffer_load_dwordx4 v[160:163], %[voff], %[srd], %[so] offen offset:0 sc1\n\t"
    "buffer_load_dwordx4 v[164:167], %[voff], %[srd], %[so] offen offset:1024 sc1\n\t"
    "buffer_load_dwordx4 v[168:171], %[voff], %[srd], %[so] offen offset:2048 sc1\n\t"
    "buffer_load_dwordx4 v[172:175], %[voff], %[srd], %[so] offen offset:3072 sc1\n\t"
    "s_add_u32 %[so], %[so], 4096\n\t"
    "buffer_load_dwordx4 v[176:179], %[voff], %[srd], %[so] offen offset:0 sc1\n\t"
    "buffer_load_dwordx4 v[180:183], %[voff], %[srd], %[so] offen offset:1024 sc1\n\t"
    "buffer_load_dwordx4 v[184:187], %[voff], %[srd], %[so] offen offset:2048 sc1\n\t"
    "buffer_load_dwordx4 v[188:191], %[voff], %[srd], %[so] offen offset:3072 sc1\n\t"
    "s_add_u32 %[so], %[so], 4096\n\t"
    "buffer_load_dwordx4 v[192:195], %[voff], %[srd], %[so] offen offset:0 sc1\n\t"
    "buffer_load_dwordx4 v[196:199], %[voff], %[srd], %[so] offen offset:1024 sc1\n\t"
    "buffer_load_dwordx4 v[200:203], %[voff], %[srd], %[so] offen offset:2048 sc1\n\t"
    "buffer_load_dwordx4 v[204:207], %[voff], %[srd], %[so] offen offset:3072 sc1\n\t"
    "s_add_u32 %[so], %[so], 4096\n\t"
    "buffer_load_dwordx4 v[208:211], %[voff], %[srd], %[so] offen offset:0 sc1\n\t"
    "buffer_load_dwordx4 v[212:215], %[voff], %[srd], %[so] offen offset:1024 sc1\n\t"
    "buffer_load_dwordx4 v[216:219], %[voff], %[srd], %[so] offen offset:2048 sc1\n\t"
    "buffer_load_dwordx4 v[220:223], %[voff], %[srd], %[so] offen offset:3072 sc1\n\t"
    "s_add_u32 %[so], %[so], 4096\n\t"
    "buffer_load_dwordx4 v[224:227], %[voff], %[srd], %[so] offen offset:0 sc1\n\t"
    "buffer_load_dwordx4 v[228:231], %[voff], %[srd], %[so] offen offset:1024 sc1\n\t"
    "buffer_load_dwordx4 v[232:235], %[voff], %[srd], %[so] offen offset:2048 sc1\n\t"
    "buffer_load_dwordx4 v[236:239], %[voff], %[srd], %[so] offen offset:3072 sc1\n\t"
    "v_mov_b32 v64, 0\n\t"
    "v_mov_b32 v65, 0\n\t"
    "v_mov_b32 v66, 0\n\t"
    "v_mov_b32 v67, 0\n\t"
    "v_mov_b32 v68, 0\n\t"
    "v_mov_b32 v69, 0\n\t"
    "v_mov_b32 v70, 0\n\t"
    "v_mov_b32 v71, 0\n\t"
    "v_mov_b32 v72, 0\n\t"
    "v_mov_b32 v73, 0\n\t"
    "v_mov_b32 v74, 0\n\t"
    "v_mov_b32 v75, 0\n\t"
    "v_mov_b32 v76, 0\n\t"
    "v_mov_b32 v77, 0\n\t"
    "v_mov_b32 v78, 0\n\t"
    "v_mov_b32 v79, 0\n\t"
    "v_mov_b32 v80, 0\n\t"
    "v_mov_b32 v81, 0\n\t"
    "v_mov_b32 v82, 0\n\t"
    "v_mov_b32 v83, 0\n\t"
    "v_mov_b32 v84, 0\n\t"
    "v_mov_b32 v85, 0\n\t"
    "v_mov_b32 v86, 0\n\t"
    "v_mov_b32 v87, 0\n\t"
    "v_mov_b32 v88, 0\n\t"
    "v_mov_b32 v89, 0\n\t"
    "v_mov_b32 v90, 0\n\t"
    "v_mov_b32 v91, 0\n\t"
    "v_mov_b32 v92, 0\n\t"
    "v_mov_b32 v93, 0\n\t"
    "v_mov_b32 v94, 0\n\t"
    "v_mov_b32 v95, 0\n\t"
                    : "=&{v[64:79]}"(y0), "=&{v[80:95]}"(y1),
                      "=&{v[128:135]}"(hz[0]), "=&{v[136:143]}"(hz[1]), "=&{v[144:151]}"(hz[2]), "=&{v[152:159]}"(hz[3]), "=&{v[160:167]}"(hz[4]), "=&{v[168:175]}"(hz[5]), "=&{v[176:183]}"(hz[6]),
                      "=&{v[184:191]}"(hz[7]), "=&{v[192:199]}"(hz[8]), "=&{v[200:207]}"(hz[9]), "=&{v[208:215]}"(hz[10]), "=&{v[216:223]}"(hz[11]), "=&{v[224:231]}"(hz[12]), "=&{v[232:239]}"(hz[13]),
                      [so] "+s"(so)
                    : [voff] "v"(hoff), [srd] "s"(srdW)
                    : "memory", "scc");
                __builtin_amdgcn_s_setprio(0);
                asm volatile(
    "ds_read_b128 v[96:99], %[ce0] offset:0\n\t"
    "ds_read_b128 v[100:103], %[ce0] offset:4096\n\t"
    "ds_read_b128 v[104:107], %[ce1] offset:0\n\t"
    "ds_read_b128 v[108:111], %[ce1] offset:4096\n\t"
    "s_waitcnt vmcnt(27) lgkmcnt(2)\n\t"
    "v_mfma_f32_32x32x16_bf16 v[64:79], v[128:131], v[96:99], v[64:79]\n\t"
    "v_mfma_f32_32x32x16_bf16 v[80:95], v[128:131], v[100:103], v[80:95]\n\t"
    "ds_read_b128 v[112:115], %[ce0] offset:8192\n\t"
    "ds_read_b128 v[116:119], %[ce0] offset:12288\n\t"
    "s_waitcnt vmcnt(26) lgkmcnt(2)\n\t"
    "v_mfma_f32_32x32x16_bf16 v[64:79], v[132:135], v[104:107], v[64:79]\n\t"
    "v_mfma_f32_32x32x16_bf16 v[80:95], v[132:135], v[108:111], v[80:95]\n\t"
    "ds_read_b128 v[96:99], %[ce1] offset:8192\n\t"
    "ds_read_b128 v[100:103], %[ce1] offset:12288\n\t"
    "s_waitcnt vmcnt(25) lgkmcnt(2)\n\t"
    "v_mfma_f32_32x32x16_bf16 v[64:79], v[136:139], v[112:115], v[64:79]\n\t"
    "v_mfma_f32_32x32x16_bf16 v[80:95], v[136:139], v[116:119], v[80:95]\n\t"
    "ds_read_b128 v[104:107], %[co0] offset:0\n\t"
    "ds_read_b128 v[108:111], %[co0] offset:4096\n\t"
    "s_waitcnt vmcnt(24) lgkmcnt(2)\n\t"
    "v_mfma_f32_32x32x16_bf16 v[64:79], v[140:143], v[96:99], v[64:79]\n\t"
    "v_mfma_f32_32x32x16_bf16 v[80:95], v[140:143], v[100:103], v[80:95]\n\t"
    "ds_read_b128 v[112:115], %[co1] offset:0\n\t"
    "ds_read_b128 v[116:119], %[co1] offset:4096\n\t"
    "s_waitcnt vmcnt(23) lgkmcnt(2)\n\t"
    "v_mfma_f32_32x32x16_bf16 v[64:79], v[144:147], v[104:107], v[64:79]\n\t"
    "v_mfma_f32_32x32x16_bf16 v[80:95], v[144:147], v[108:111], v[80:95]\n\t"
    "ds_read_b128 v[96:99], %[co0] offset:8192\n\t"
    "ds_read_b128 v[100:103], %[co0] offset:12288\n\t"
    "s_waitcnt vmcnt(22) lgkmcnt(2)\n\t"
    "v_mfma_f32_32x32x16_bf16 v[64:79], v[148:151], v[112:115], v[64:79]\n\t"
    "v_mfma_f32_32x32x16_bf16 v[80:95], v[148:151], v[116:119], v[80:95]\n\t"
    "ds_read_b128 v[104:107], %[co1] offset:8192\n\t"
    "ds_read_b128 v[108:111], %[co1] offset:12288\n\t"
    "s_waitcnt vmcnt(21) lgkmcnt(2)\n\t"
    "v_mfma_f32_32x32x16_bf16 v[64:79], v[152:155], v[96:99], v[64:79]\n\t"
    "v_mfma_f32_32x32x16_bf16 v[80:95], v[152:155], v[100:103], v[80:95]\n\t"
    "ds_read_b128 v[112:115], %[ce0] offset:16384\n\t"
    "ds_read_b128 v[116:119], %[ce0] offset:20480\n\t"
    "s_waitcnt vmcnt(20) lgkmcnt(2)\n\t"
    "v_mfma_f32_32x32x16_bf16 v[64:79], v[156:159], v[104:107], v[64:79]\n\t"
    "v_mfma_f32_32x32x16_bf16 v[80:95], v[156:159], v[108:111], v[80:95]\n\t"
    "ds_read_b128 v[96:99], %[ce1] offset:16384\n\t"
    "ds_read_b128 v[100:103], %[ce1] offset:20480\n\t"
    "s_waitcnt vmcnt(19) lgkmcnt(2)\n\t"
    "v_mfma_f32_32x32x16_bf16 v[64:79], v[160:163], v[112:115], v[64:79]\n\t"
    "v_mfma_f32_32x32x16_bf16 v[80:95], v[160:163], v[116:119], v[80:95]\n\t"
    "ds_read_b128 v[104:107], %[ce0] offset:24576\n\t"
    "ds_read_b128 v[108:111], %[ce0] offset:28672\n\t"
    "s_waitcnt vmcnt(18) lgkmcnt(2)\n\t"
    "v_mfma_f32_32x32x16_bf16 v[64:79], v[164:167], v[96:99], v[64:79]\n\t"
    "v_mfma_f32_32x32x16_bf16 v[80:95], v[164:167], v[100:103], v[80:95]\n\t"
    "ds_read_b128 v[112:115], %[ce1] offset:24576\n\t"
    "ds_read_b128 v[116:119], %[ce1] offset:28672\n\t"
    "s_waitcnt vmcnt(17) lgkmcnt(2)\n\t"
    "v_mfma_f32_32x32x16_bf16 v[64:79], v[168:171], v[104:107], v[64:79]\n\t"
    "v_mfma_f32_32x32x16_bf16 v[80:95], v[168:171], v[108:111], v[80:95]\n\t"
    "ds_read_b128 v[96:99], %[co0] offset:16384\n\t"
    "ds_read_b128 v[100:103], %[co0] offset:20480\n\t"
    "s_waitcnt vmcnt(16) lgkmcnt(2)\n\t"
    "v_mfma_f32_32x32x16_bf16 v[64:79], v[172:175], v[112:115], v[64:79]\n\t"
    "v_mfma_f32_32x32x16_bf16 v[80:95], v[172:175], v[116:119], v[80:95]\n\t"
    "ds_read_b128 v[104:107], %[co1] offset:16384\n\t"
    "ds_read_b128 v[108:111], %[co1] offset:20480\n\t"
    "s_waitcnt vmcnt(15) lgkmcnt(2)\n\t"
    "v_mfma_f32_32x32x16_bf16 v[64:79], v[176:179], v[96:99], v[64:79]\n\t"
    "v_mfma_f32_32x32x16_bf16 v[80:95], v[176:179], v[100:103], v[80:95]\n\t"
    "ds_read_b128 v[112:115], %[co0] offset:24576\n\t"
    "ds_read_b128 v[116:119], %[co0] offset:28672\n\t"
    "s_waitcnt vmcnt(14) lgkmcnt(2)\n\t"
    "v_mfma_f32_32x32x16_bf16 v[64:79], v[180:183], v[104:107], v[64:79]\n\t"
    "v_mfma_f32_32x32x16_bf16 v[80:95], v[180:183], v[108:111], v[80:95]\n\t"
    "ds_read_b128 v[96:99], %[co1] offset:24576\n\t"
    "ds_read_b128 v[100:103], %[co1] offset:28672\n\t"
    "s_waitcnt vmcnt(13) lgkmcnt(2)\n\t"
    "v_mfma_f32_32x32x16_bf16 v[64:79], v[184:187], v[112:115], v[64:79]\n\t"
    "v_mfma_f32_32x32x16_bf16 v[80:95], v[184:187], v[116:119], v[80:95]\n\t"
    "ds_read_b128 v[104:107], %[ce0] offset:32768\n\t"
    "ds_read_b128 v[108:111], %[ce0] offset:36864\n\t"
    "s_waitcnt vmcnt(12) lgkmcnt(2)\n\t"
    "v_mfma_f32_32x32x16_bf16 v[64:79], v[188:191], v[96:99], v[64:79]\n\t"
    "v_mfma_f32_32x32x16_bf16 v[80:95], v[188:191], v[100:103], v[80:95]\n\t"
    "ds_read_b128 v[112:115], %[ce1] offset:32768\n\t"
    "ds_read_b128 v[116:119], %[ce1] offset:36864\n\t"
    "s_waitcnt vmcnt(11) lgkmcnt(2)\n\t"
    "v_mfma_f32_32x32x16_bf16 v[64:79], v[192:195], v[104:107], v[64:79]\n\t"
    "v_mfma_f32_32x32x16_bf16 v[80:95], v[192:195], v[108:111], v[80:95]\n\t"
    "ds_read_b128 v[96:99], %[ce0] offset:40960\n\t"
    "ds_read_b128 v[100:103], %[ce0] offset:45056\n\t"
    "s_waitcnt vmcnt(10) lgkmcnt(2)\n\t"
    "v_mfma_f32_32x32x16_bf16 v[64:79], v[196:199], v[112:115], v[64:79]\n\t"
    "v_mfma_f32_32x32x16_bf16 v[80:95], v[196:199], v[116:119], v[80:95]\n\t"
    "ds_read_b128 v[104:107], %[ce1] offset:40960\n\t"
    "ds_read_b128 v[108:111], %[ce1] offset:45056\n\t"
    "s_waitcnt vmcnt(9) lgkmcnt(2)\n\t"
    "v_mfma_f32_32x32x16_bf16 v[64:79], v[200:203], v[96:99], v[64:79]\n\t"
    "v_mfma_f32_32x32x16_bf16 v[80:95], v[200:203], v[100:103], v[80:95]\n\t"
    "ds_read_b128 v[112:115], %[co0] offset:32768\n\t"
    "ds_read_b128 v[116:119], %[co0] offset:36864\n\t"
    "s_waitcnt vmcnt(8) lgkmcnt(2)\n\t"
    "v_mfma_f32_32x32x16_bf16 v[64:79], v[204:207], v[104:107], v[64:79]\n\t"
    "v_mfma_f32_32x32x16_bf16 v[80:95], v[204:207], v[108:111], v[80:95]\n\t"
    "ds_read_b128 v[96:99], %[co1] offset:32768\n\t"
    "ds_read_b128 v[100:103], %[co1] offset:36864\n\t"
    "s_waitcnt vmcnt(7) lgkmcnt(2)\n\t"
    "v_mfma_f32_32x32x16_bf16 v[64:79], v[208:211], v[112:115], v[64:79]\n\t"
    "v_mfma_f32_32x32x16_bf16 v[80:95], v[208:211], v[116:119], v[80:95]\n\t"
    "ds_read_b128 v[104:107], %[co0] offset:40960\n\t"
    "ds_read_b128 v[108:111], %[co0] offset:45056\n\t"
    "s_waitcnt vmcnt(6) lgkmcnt(2)\n\t"
    "v_mfma_f32_32x32x16_bf16 v[64:79], v[212:215], v[96:99], v[64:79]\n\t"
    "v_mfma_f32_32x32x16_bf16 v[80:95], v[212:215], v[100:103], v[80:95]\n\t"
    "ds_read_b128 v[112:115], %[co1] offset:40960\n\t"
    "ds_read_b128 v[116:119], %[co1] offset:45056\n\t"
    "s_waitcnt vmcnt(5) lgkmcnt(2)\n\t"
    "v_mfma_f32_32x32x16_bf16 v[64:79], v[216:219], v[104:107], v[64:79]\n\t"
    "v_mfma_f32_32x32x16_bf16 v[80:95], v[216:219], v[108:111], v[80:95]\n\t"
    "ds_read_b128 v[96:99], %[ce0] offset:49152\n\t"
    "ds_read_b128 v[100:103], %[ce0] offset:53248\n\t"
    "s_waitcnt vmcnt(4) lgkmcnt(2)\n\t"
    "v_mfma_f32_32x32x16_bf16 v[64:79], v[220:223], v[112:115], v[64:79]\n\t"
    "v_mfma_f32_32x32x16_bf16 v[80:95], v[220:223], v[116:119], v[80:95]\n\t"
    "ds_read_b128 v[104:107], %[ce1] offset:49152\n\t"
    "ds_read_b128 v[108:111], %[ce1] offset:53248\n\t"
    "s_waitcnt vmcnt(3) lgkmcnt(2)\n\t"
    "v_mfma_f32_32x32x16_bf16 v[64:79], v[224:227], v[96:99], v[64:79]\n\t"
    "v_mfma_f32_32x32x16_bf16 v[80:95], v[224:227], v[100:103], v[80:95]\n\t"
    "ds_read_b128 v[112:115], %[ce0] offset:57344\n\t"
    "ds_read_b128 v[116:119], %[ce0] offset:61440\n\t"
    "s_waitcnt vmcnt(2) lgkmcnt(2)\n\t"
    "v_mfma_f32_32x32x16_bf16 v[64:79], v[228:231], v[104:107], v[64:79]\n\t"
    "v_mfma_f32_32x32x16_bf16 v[80:95], v[228:231], v[108:111], v[80:95]\n\t"
    "ds_read_b128 v[96:99], %[ce1] offset:57344\n\t"
    "ds_read_b128 v[100:103], %[ce1] offset:61440\n\t"
    "s_waitcnt vmcnt(1) lgkmcnt(2)\n\t"
    "v_mfma_f32_32x32x16_bf16 v[64:79], v[232:235], v[112:115], v[64:79]\n\t"
    "v_mfma_f32_32x32x16_bf16 v[80:95], v[232:235], v[116:119], v[80:95]\n\t"
    "s_waitcnt vmcnt(0) lgkmcnt(0)\n\t"
    "v_mfma_f32_32x32x16_bf16 v[64:79], v[236:239], v[96:99], v[64:79]\n\t"
    "v_mfma_f32_32x32x16_bf16 v[80:95], v[236:239], v[100:103], v[80:95]\n\t"
    "s_nop 7\n\t"
    "s_nop 7\n\t"
                    : "+{v[64:79]}"(y0), "+{v[80:95]}"(y1),
                      "+{v[128:135]}"(hz[0]), "+{v[136:143]}"(hz[1]), "+{v[144:151]}"(hz[2]), "+{v[152:159]}"(hz[3]), "+{v[160:167]}"(hz[4]), "+{v[168:175]}"(hz[5]), "+{v[176:183]}"(hz[6]),
                      "+{v[184:191]}"(hz[7]), "+{v[192:199]}"(hz[8]), "+{v[200:207]}"(hz[9]), "+{v[208:215]}"(hz[10]), "+{v[216:223]}"(hz[11]), "+{v[224:231]}"(hz[12]), "+{v[232:239]}"(hz[13])
                    : [ce0] "v"(ce0), [ce1] "v"(ce1), [co0] "v"(co0), [co1] "v"(co1)
                    : "memory", "v96", "v97", "v98", "v99", "v100", "v101", "v102", "v103", "v104", "v105", "v106", "v107", "v108", "v109", "v110", "v111", "v112", "v113", "v114", "v115", "v116", "v117", "v118", "v119");
            }

            __builtin_amdgcn_s_setprio(2);
            const lptr rbuf = lds + L_HT;
            f32x16 y;
            if (kh) {
                UNIFORM_BRANCH();
#pragma unroll
                for (int q4 = 0; q4 < 4; ++q4) *(LAS f32x4*)(rbuf + (sw * 4 + q4) * 1024 + lane * 16) = (f32x4){y0[4 * q4], y0[4 * q4 + 1], y0[4 * q4 + 2], y0[4 * q4 + 3]};
                y = y1;
            } else {
#pragma unroll
                for (int q4 = 0; q4 < 4; ++q4) *(LAS f32x4*)(rbuf + (sw * 4 + q4) * 1024 + lane * 16) = (f32x4){y1[4 * q4], y1[4 * q4 + 1], y1[4 * q4 + 2], y1[4 * q4 + 3]};
                y = y0;
            }
            sbar<2>(cntw, cntr, sepoch);
            {
                const i32x4 srdO = make_srd(Wo, DM * DM * 2);
                const int row = 8 * sw + (lane >> 3);
                const unsigned vo = (unsigned)(row * 896 + db * 128 + (((lane & 7) ^ (((row >> 1) & 1) << 2)) << 4));
#pragma unroll 1
                for (int i = 0; i < 14; ++i) dma16(srdO, vo, i * 32 * 896, ldsb + L_WO + (4 * i + sw) * 1024);
                if (sw == 0) dma16(srdO, 0x7fffffffu, 0, ldsb + L_WO + 56 * 1024);
            }
            const float dsk = bf2f((u16)dskw);
            const __amdgpu_buffer_rsrc_t outr = __builtin_amdgcn_make_buffer_rsrc(out, 0, SEQ * OUTW * 4, 0x00020000);
            const unsigned oo = (unsigned)(((32 * tt + 4 * hi) * OUTW + dcol) * 4);
#pragma unroll
            for (int q4 = 0; q4 < 4; ++q4) {
                const f32x4 o4 = *(const LAS f32x4*)(rbuf + ((sw ^ 2) * 4 + q4) * 1024 + lane * 16);
#pragma unroll
                for (int e4 = 0; e4 < 4; ++e4) {
                    const int r = 4 * q4 + e4;
                    const float v = (y[r] + o4[e4]) + rbf(bf2f((u16)xs[r]) * dsk);
                    __builtin_amdgcn_raw_buffer_store_b32(F2U(v), outr, oo, (e4 + 8 * q4) * OUTW * 4, 16);
                }
            }
        }
        __builtin_amdgcn_s_setprio(0);

        WAIT_BAR(0);
        BAR_LGKM();
      }
    }

    if (wave < 4) {

#pragma unroll
        for (int i = 0; i < 4; ++i) {
            const int dst = (wave + i) & 3;
            if (hi) *(LAS f32x2*)(lds + L_LM + ((dst * 4 + wave) * 32 + r32) * 8) = (f32x2){O[i][12], meff[i]};
            if (i > 0) {
                *(LAS bf16x8*)(lds + L_CMB + ((dst * 3 + 3 - i) * 2 + 0) * 1024 + lane * 16) = pack8(O[i], 0);
                *(LAS bf16x8*)(lds + L_CMB + ((dst * 3 + 3 - i) * 2 + 1) * 1024 + lane * 16) = pack8(O[i], 1);
            }
        }
        sbar<0>(aw_, ar_, aepoch);

        {
            f32x2 lm[4]; u32x4 pp[3][2];
#pragma unroll
            for (int k = 0; k < 4; ++k) lm[k] = *(const LAS f32x2*)(lds + L_LM + ((wave * 4 + ((wave + k) & 3)) * 32 + r32) * 8);
#pragma unroll
            for (int src = 0; src < 3; ++src) { pp[src][0] = *(const LAS u32x4*)(lds + L_CMB + ((wave * 3 + src) * 2 + 0) * 1024 + lane * 16); pp[src][1] = *(const LAS u32x4*)(lds + L_CMB + ((wave * 3 + src) * 2 + 1) * 1024 + lane * 16); }
            const float M = __builtin_fmaxf(__builtin_fmaxf(lm[0][1], lm[1][1]), __builtin_fmaxf(lm[2][1], lm[3][1]));
            float f[4], l = 0.f;
#pragma unroll
            for (int k = 0; k < 4; ++k) { f[k] = __builtin_amdgcn_exp2f(lm[k][1] - M); l = __builtin_fmaf(f[k], lm[k][0], l); }
            const float inv = __builtin_amdgcn_rcpf(l);
            f32x16 own = O[0];
            const float fo = f[0] * inv;
#pragma unroll
            for (int r = 0; r < 16; ++r) own[r] *= fo;
#pragma unroll
            for (int src = 0; src < 3; ++src) {
                const float fs = f[src + 1] * inv;
#pragma unroll
                for (int r = 0; r < 16; r += 2) {
                    const unsigned v = pp[src][r >> 3][(r & 7) >> 1];
                    own[r] = __builtin_fmaf(fs, U2F(v << 16), own[r]); own[r + 1] = __builtin_fmaf(fs, U2F(v & 0xffff0000u), own[r + 1]);
                }
            }
            if (hi) { own[12] = 0.f; own[13] = 0.f; own[14] = 0.f; own[15] = 0.f; }
            const unsigned oof = (unsigned)(WS_OF + (((4 * rb + wave) * 16 + head) * 2) * 1024 + lane * 16);
            st16_sc1(wsr, oof, __builtin_bit_cast(u32x4, pack8(own, 0))); st16_sc1(wsr, oof + 1024, __builtin_bit_cast(u32x4, pack8(own, 1)));
        }

        WAIT_VM(0);
        if (lane == 0) {
            if (__hip_atomic_fetch_add((LAS unsigned*)(lds + L_ACNT + 4), 1u, __ATOMIC_RELAXED, __HIP_MEMORY_SCOPE_WORKGROUP) == 3)
                __hip_atomic_store(flags + 32 * (F_O + rb) + head, tag, RLX_AGENT);
        }

        if (b < 224) {
            f32x16 y0 = {}, y1 = {};
            const int tt = 2 * tb + (wave & 1), kh = (wave >> 1) & 1;
            asm volatile("s_barrier" ::: "memory");
            bf16x8 wf0[16], wf1[16];
            {
                const int q = (lane & 15) >> 2, p = lane & 3, blk = (lane >> 4) & 1;
                const int sz = ((q >> 1) & 1) << 2, c16 = 2 * blk + (p >> 1);
                const lptr wb = lds + L_WO + 8 * (p & 1) + (8 * kh * 28 + 4 * hi + q) * 128;
                const lptr w0 = wb + ((c16 ^ sz) << 4), w1 = wb + (((c16 + 4) ^ sz) << 4);
#pragma unroll
                for (int f = 0; f < 16; ++f) {
                    const int ro = ((f >> 1) * 28 + 16 * (f & 1)) * 128;
                    wf0[f] = lds_tr2(w0 + ro, w0 + ro + 1024); wf1[f] = lds_tr2(w1 + ro, w1 + ro + 1024);
                }
#pragma unroll
                for (int f = 0; f < 16; f += 4)
                    asm volatile("" : "+v"(wf0[f]), "+v"(wf0[f + 1]), "+v"(wf0[f + 2]), "+v"(wf0[f + 3]), "+v"(wf1[f]), "+v"(wf1[f + 1]), "+v"(wf1[f + 2]), "+v"(wf1[f + 3]));
            }
            group_wait<0>(flags + 32 * (F_O + (tb >> 1)) + (lane & 15), lane < 16, tag, tmo, wave == 0, lds + L_ACNT + 20);

            const unsigned ooff = (unsigned)(WS_OF + (tt * 32 + 16 * kh) * 1024 + lane * 16);
            u32x4 oa[16];
#pragma unroll
            for (int f = 0; f < 16; ++f) oa[f] = ld16_sc1(wsr, ooff, f * 1024);
            SCHED_FENCE();
#pragma unroll
            for (int f = 0; f < 16; ++f) {
                const bf16x8 a = __builtin_bit_cast(bf16x8, oa[f]);
                y0 = MFMA(a, wf0[f], y0); y1 = MFMA(a, wf1[f], y1);
            }

            const lptr xb = lds + L_XCH + (wave & 1) * 16384;
            {
                const lptr wp = xb + kh * 4096 + (4 * hi) * 128 + r32 * 4;
#pragma unroll
                for (int r = 0; r < 16; ++r) {
                    *(LAS float*)(wp + ((r & 3) + 8 * (r >> 2)) * 128) = y0[r];
                    *(LAS float*)(wp + 8192 + ((r & 3) + 8 * (r >> 2)) * 128) = y1[r];
                }
            }
            BAR_LGKM();
            const __amdgpu_buffer_rsrc_t outr = __builtin_amdgcn_make_buffer_rsrc(out, 0, SEQ * OUTW * 4, 0x00020000);
            const unsigned oo = (unsigned)(((32 * tt + (lane >> 3)) * OUTW + 448 + 64 * db + 32 * kh) * 4 + (lane & 7) * 16);
            const lptr rp = xb + kh * 8192 + lane * 16;
            f32x4 pa[4], pb[4];
#pragma unroll
            for (int i = 0; i < 4; ++i) { pa[i] = *(const LAS f32x4*)(rp + i * 1024); pb[i] = *(const LAS f32x4*)(rp + 4096 + i * 1024); }
#pragma unroll
            for (int i = 0; i < 4; ++i) {
                const u32x4 v = {cvtpk(0.f, pa[i][0] + pb[i][0]), cvtpk(0.f, pa[i][1] + pb[i][1]), cvtpk(0.f, pa[i][2] + pb[i][2]), cvtpk(0.f, pa[i][3] + pb[i][3])};
                __builtin_amdgcn_raw_buffer_store_b128(v, outr, oo, i * 8 * OUTW * 4, 16);
            }
        }
    }

    if (b == 255 && wave == 7) {
        GAS u64* p = flags + 32 * (F_O + (lane >> 4)) + (lane & 15);
#pragma unroll 1
        for (unsigned spins = 0;; ++spins) {
            const u64 f0 = __hip_atomic_load(p, RLX_AGENT), f1 = __hip_atomic_load(p + 128, RLX_AGENT), f2 = __hip_atomic_load(p + 256, RLX_AGENT), f3 = __hip_atomic_load(p + 384, RLX_AGENT);
            if (__builtin_amdgcn_ballot_w64(f0 != tag || f1 != tag || f2 != tag || f3 != tag) == 0) break;
            if (spins > SPIN_CAP) { __hip_atomic_store(tmo, 1u, RLX_AGENT); break; }
            __builtin_amdgcn_s_sleep(32);
        }
        if (lane == 0) __hip_atomic_store((GAS u64*)(ws + WS_EP), tag, RLX_AGENT);
    }
}

extern "C" void kernel_launch(void* const* d_in, const int* in_sizes, int n_in, void* d_out, int out_size, void* d_ws, size_t ws_size, hipStream_t stream) {
    (void)in_sizes; (void)n_in; (void)out_size; (void)ws_size;
    (void)hipFuncSetAttribute((const void*)fused_kernel, hipFuncAttributeMaxDynamicSharedMemorySize, LDS_BYTES);
    fused_kernel<<<256, 512, LDS_BYTES, stream>>>(
        (const u16*)d_in[0], (const u16*)d_in[1], (const u16*)d_in[2], (const u16*)d_in[3], (const u16*)d_in[4], (const u16*)d_in[5], (const u16*)d_in[6],
        (const u16*)d_in[7], (const u16*)d_in[8], (const u16*)d_in[9], (const u16*)d_in[10], (const u16*)d_in[11], (float*)d_out, (char*)d_ws);
}
```
